```python
import torch

def _softmax(x, axis=-1):
    return torch.softmax(x, dim=axis)

def _gelu(x):
    return torch.nn.functional.gelu(x, approximate='tanh')

def _silu(x):
    return torch.nn.functional.silu(x)

def _sigmoid(x):
    return torch.sigmoid(x)

def _layernorm(x, axis=-1, eps=1e-05):
    m = x.mean(dim=axis, keepdim=True)
    v = x.var(dim=axis, keepdim=True, unbiased=False)
    return (x - m) / torch.sqrt(v + eps)

def _scatter_add(tgt, idx, src, axis=0):
    return tgt.clone().index_add_(axis, idx, src)

def _segment_sum(src, seg, n):
    out = torch.zeros((n,) + src.shape[1:], dtype=src.dtype)
    return out.index_add_(0, seg, src)

def _pad(x, pw):
    flat = tuple((v for pair in reversed(pw) for v in pair))
    return torch.nn.functional.pad(x, flat)
kernel_name = 'standalone__kv_cache_quant_pre_rope_vs_post_rope__prompt'

def setup_inputs():

    _w = lambda a: (lambda t: t.to(torch.bfloat16) if t.is_floating_point() else t)(torch.as_tensor(a))
    B, Sq, Skv = (1, 2048, 2048)
    H, Dh0 = (8, 128)
    G = -1 if -1 > 0 else Dh0
    G = max(2, G // 2 * 2)
    Dh = max(G, Dh0 // G * G)
    Skv = max(Skv, Sq)
    ng = Dh // G
    bits = 2 if 2 > 1 else 4
    bits = max(2, min(8, bits))
    qmax = (1 << bits) - 1
    half = Dh // 2
    inv = 1.0 / 10000.0 ** (torch.arange(half) / max(1, half))
    ang_k = torch.arange(Skv)[:, None] * inv[None, :]
    ang_q = torch.arange(Skv - Sq, Skv)[:, None] * inv[None, :]
    kc = torch.cat([torch.cos(ang_k), torch.cos(ang_k)], dim=-1)
    ks = torch.cat([torch.sin(ang_k), torch.sin(ang_k)], dim=-1)
    qc = torch.cat([torch.cos(ang_q), torch.cos(ang_q)], dim=-1)
    qs = torch.cat([torch.sin(ang_q), torch.sin(ang_q)], dim=-1)
    qk_i = torch.randint(0, qmax + 1, (B, H, Skv, Dh)).to(torch.int32)
    qv_i = torch.randint(0, qmax + 1, (B, H, Skv, Dh)).to(torch.int32)
    ksc = torch.abs((lambda *s: torch.randn(*s, dtype=torch.bfloat16) * 0.4)(B, H, Skv, ng)) * 0.1 + 0.01
    kzp = torch.full((B, H, Skv, ng), qmax / 2.0)
    vsc = torch.abs((lambda *s: torch.randn(*s, dtype=torch.bfloat16) * 0.4)(B, H, Skv, ng)) * 0.1 + 0.01
    vzp = torch.full((B, H, Skv, ng), qmax / 2.0)
    return {'q': _w((lambda *s: torch.randn(*s, dtype=torch.bfloat16) * 6.4)(B, H, Sq, Dh) * 0.5), 'qk': _w(qk_i), 'k_scales': _w(ksc), 'k_zeros': _w(kzp), 'qv': _w(qv_i), 'v_scales': _w(vsc), 'v_zeros': _w(vzp), 'q_cos': _w(qc), 'q_sin': _w(qs), 'k_cos': _w(kc), 'k_sin': _w(ks)}

def reference(q, qk, k_scales, k_zeros, qv, v_scales, v_zeros, q_cos, q_sin, k_cos, k_sin):
    B, H, Sq, Dh = q.shape
    ng = k_scales.shape[-1]
    G = Dh // ng
    half = Dh // 2
    ks = (lambda x, n, axis: torch.repeat_interleave(x, n, dim=axis))(k_scales, G, axis=-1)
    kz = (lambda x, n, axis: torch.repeat_interleave(x, n, dim=axis))(k_zeros, G, axis=-1)
    k = ((lambda x, dt: x.to(dt))(qk, q.dtype) - kz) * ks
    vs = (lambda x, n, axis: torch.repeat_interleave(x, n, dim=axis))(v_scales, G, axis=-1)
    vz = (lambda x, n, axis: torch.repeat_interleave(x, n, dim=axis))(v_zeros, G, axis=-1)
    v = ((lambda x, dt: x.to(dt))(qv, q.dtype) - vz) * vs
    k1 = k[..., :half].clone()
    k2 = k[..., half:].clone()
    krh = torch.concatenate([-k2, k1], axis=-1)
    kr = k * k_cos[None, None, :, :] + krh * k_sin[None, None, :, :]
    q1 = q[..., :half].clone()
    q2 = q[..., half:].clone()
    qrh = torch.concatenate([-q2, q1], axis=-1)
    qr = q * q_cos[None, None, :, :] + qrh * q_sin[None, None, :, :]
    logits = torch.einsum('bhqd,bhkd->bhqk', qr, kr) * float(Dh) ** (-0.5)
    attn = _softmax(logits, axis=-1)
    out = torch.einsum('bhqk,bhkd->bhqd', attn, v)
    return out
```

```cpp
#include <hip/hip_runtime.h>
#include <hip/hip_bf16.h>
#include <stdint.h>

typedef float    f32x16 __attribute__((ext_vector_type(16)));
typedef float    f32x4  __attribute__((ext_vector_type(4)));
typedef float    f32x2  __attribute__((ext_vector_type(2)));
typedef __bf16   bf16x2 __attribute__((ext_vector_type(2)));
typedef unsigned u32x16 __attribute__((ext_vector_type(16)));
typedef unsigned u32x4  __attribute__((ext_vector_type(4)));
typedef unsigned u32x2  __attribute__((ext_vector_type(2)));
typedef int      i32x4  __attribute__((ext_vector_type(4)));
typedef __attribute__((address_space(1))) unsigned long long gu64;
#define DI __device__ __forceinline__
#define SB() __builtin_amdgcn_sched_barrier(0)

constexpr int H = 8, S = 2048, D = 128;
constexpr int NT = S / 32;
constexpr unsigned KV_TILE_B = 16384;
constexpr unsigned KV_BYTES = H * NT * KV_TILE_B;
constexpr unsigned FLAG_OFF = KV_BYTES;
constexpr unsigned EPOCH_OFF = FLAG_OFF + 32768;
constexpr int EX_LDS = 8 * 12544 + 256, Q_LDS = EX_LDS + 4 * 3072, XP_LDS = Q_LDS + 16 * 1056;
constexpr int LDS_B = XP_LDS + 4096;

DI unsigned cvtpk(float lo, float hi) { f32x2 v = {lo, hi}; bf16x2 b = __builtin_convertvector(v, bf16x2); return __builtin_bit_cast(unsigned, b); }
DI float bflo(unsigned u) { return __uint_as_float(u << 16); }
DI float bfhi(unsigned u) { return __uint_as_float(u & 0xffff0000u); }
DI float bf2f(unsigned short u) { return __uint_as_float((unsigned)u << 16); }
DI f32x2 bfpair(unsigned u) { return f32x2{bflo(u), bfhi(u)}; }
DI unsigned rfl(unsigned x) { return __builtin_amdgcn_readfirstlane(x); }
DI f32x2 fma2(f32x2 a, f32x2 b, f32x2 c) { return __builtin_elementwise_fma(a, b, c); }

#define CLOB_V "v4","v5","v6","v7","v24","v25","v26","v27","v28","v29","v30","v31","v32","v33","v34","v35","v36","v37","v38","v39","v40","v41","v42","v43","v44","v45","v46","v47","v48","v49","v50","v51","v52","v53","v54","v55","v56","v57","v58","v59","v60","v61","v62","v63","v96","v97","v98","v99","v100","v101","v102","v103","v104","v105","v106","v107","v108","v109","v110","v111","v112","v113","v114","v115","v116","v117","v118","v119","v120","v121","v122","v123","v124","v125","v126","v127","v128","v129","v130","v131","v132","v133","v134","v135","v136","v137","v138","v139","v140","v141","v142","v143","v144","v145","v146","v147","v148","v149","v150","v151","v152","v153","v154","v155","v156","v157","v158","v159","v160","v161","v162","v163","v164","v165","v166","v167","v168","v169","v170","v171","v172","v173","v174","v175","v176","v177","v178","v179","v180","v181","v182","v183","v184","v185","v186","v187","v188","v189","v190","v191","v192","v193","v194","v195","v196","v197","v198","v199","v200","v201","v202","v203","v204","v205","v206","v207","v208","v209","v210","v211","v212","v213","v214","v216","v217","v218","v219","v221","v222","v223","v225","v227","v248","v249"
#define CLOB_A "a5","a6","a7","a8","a9","a10","a11","a12","a13","a14","a15","a16","a17","a18","a19","a20","a21","a22","a23","a24","a25","a26","a27","a28","a29","a30","a31","a32","a33","a34","a35","a36","a37","a38","a39","a40","a41","a42","a43","a44","a45","a46","a47","a48","a49","a50","a51","a52","a53","a54","a55","a56","a57","a58","a59","a60","a61","a62","a63","a64","a65","a66","a67","a68","a69","a70","a71","a72","a73","a74","a75","a76","a77","a78","a79","a80","a81","a82","a83","a84","a85","a86","a87","a88","a89","a90","a91","a92","a93","a94","a95","a96","a97","a98","a99","a100","a101","a102","a103","a104","a105","a106","a107","a108","a109","a110","a111","a112","a113","a114","a115","a116","a117","a118","a119","a120","a121","a122","a123","a124","a125","a126","a127","a128","a129","a130","a131","a132","a133","a134","a135","a136","a137","a138","a139","a140","a141","a142","a143","a144","a145","a146","a147","a148","a149","a150","a151","a152","a153","a154","a155","a156","a157","a158","a159","a160","a161","a162","a163","a164","a165","a166","a167","a168","a169","a170","a171","a172","a173","a174","a175","a176","a177","a178","a179","a180","a181","a182","a183","a184","a185","a186","a187","a188","a189","a190","a191","a192","a193","a194","a195","a196","a197","a198","a199","a200","a201","a202","a203","a204","a205","a206","a207","a208","a209","a210","a211","a212","a213","a214","a215","a216","a217","a218","a219","a220","a221","a222","a223","a224","a225","a226","a227","a228","a229","a230","a231","a232","a233","a234","a235","a236","a237","a238","a239","a240","a241","a242","a243","a244","a245","a246","a247","a248","a249","a250","a251","a252","a253","a254","a255"
#define CLOB_S "s30","s31","s24","s25","s26","s27","s41","s42","s43","s44","s45","s46","s47","s48","s53","s54","s55","s57","s58","s77","s78","s79"
#define ROTM 0
#define GG 2

__global__ __launch_bounds__(256, 1) void fused_kernel(const unsigned short* __restrict__ q, const int* __restrict__ qk, const unsigned short* __restrict__ ksc,
    const unsigned short* __restrict__ kzp, const int* __restrict__ qv, const unsigned short* __restrict__ vsc, const unsigned short* __restrict__ vzp,
    const unsigned short* __restrict__ qcos, const unsigned short* __restrict__ qsin, const unsigned short* __restrict__ kcos, const unsigned short* __restrict__ ksin,
    char* ws, unsigned short* __restrict__ out) {
  extern __shared__ __attribute__((aligned(16))) char lds[];
  u32x16 ka0; u32x4 ka1, ka2; unsigned long long outp;
  asm volatile("s_load_dwordx16 %0, %4, 0x0\n s_load_dwordx4 %1, %4, 0x40\n s_load_dwordx4 %2, %4, 0x50\n s_load_dwordx2 %3, %4, 0x60\n"
               : "={s[80:95]}"(ka0), "={s[96:99]}"(ka1), "={s[60:63]}"(ka2), "={s[28:29]}"(outp) : "s"(__builtin_amdgcn_kernarg_segment_ptr()));
  SB();
  unsigned b = blockIdx.x, tid = threadIdx.x, w = rfl(tid >> 6);
#define SEL(a, b) ((((unsigned)(a)) & vm) | (((unsigned)(b)) & ~vm))
  u32x4 sraw, pa, f0; u32x16 sA, f1, f2, f3; u32x4 sB, sW;
  {
    const unsigned lane = tid & 63, g = (b >> 3) & 1, rho = 16 * (b & 7) + (b >> 4), T = rho >> 1, vm = 0u - (w >> 1);
    #pragma unroll
    for (unsigned e = 0; e < 4; ++e) sraw[e] = (4 * g + ((T + SEL((w & 1) + 2 * (e & 1), e)) & 3)) << 20;
    const unsigned j = lane >> 3, il = lane & 7, krow = 16 * rho + 8 * (j >> 2) + 4 * (w & 1) + (j & 3), vrow = 16 * rho + 8 * (lane >> 5);
    pa[0] = SEL(vrow * 512 + (lane & 31) * 16, krow * 512 + il * 16);
    pa[1] = krow * 256 + il * 8;
    pa[2] = SEL(vrow * 2, ((4 * g + ((T + il) & 3)) * S + krow) * 2);
    pa[3] = 0;
  }
  asm volatile(
  "s_mov_b32 s6, 8388608\n"
  "s_mov_b32 s7, 0x20000\n"
  "s_mov_b32 s10, 32768\n"
  "s_mov_b32 s11, 0x20000\n"
  "s_mov_b32 s14, 32768\n"
  "s_mov_b32 s15, 0x20000\n"
  "s_mov_b32 s18, 524288\n"
  "s_mov_b32 s19, 0x20000\n"
  "s_mov_b32 s22, 524288\n"
  "s_mov_b32 s23, 0x20000\n"
  "s_mov_b32 s38, 8421632\n"
  "s_mov_b32 s39, 0x20000\n"
  "s_lshr_b32 s78, s64, 8\n"
  "s_cmp_ge_u32 s49, 2\n"
  "s_waitcnt lgkmcnt(0)\n"
  "s_mov_b64 s[36:37], s[62:63]\n"
  "s_cselect_b64 s[8:9], s[90:91], s[84:85]\n"
  "s_cselect_b64 s[12:13], s[92:93], s[86:87]\n"
  "s_cselect_b64 s[4:5], s[88:89], s[82:83]\n"
  "buffer_load_dwordx2 v[0:1], off, s[36:39], s77 sc1\n"
  "s_cbranch_scc0 L_kinit_%=\n"
  "buffer_load_dwordx4 v[8:11], v238, s[8:11], s78 offen\n"
  "buffer_load_dwordx4 v[12:15], v238, s[12:15], s78 offen\n"
  "buffer_load_dwordx4 v[64:67], v236, s[4:7], s64 offen offset:0 nt\n"
  "buffer_load_dwordx4 v[68:71], v236, s[4:7], s64 offen offset:512 nt\n"
  "buffer_load_dwordx4 v[72:75], v236, s[4:7], s64 offen offset:1024 nt\n"
  "buffer_load_dwordx4 v[76:79], v236, s[4:7], s64 offen offset:1536 nt\n"
  "buffer_load_dwordx4 v[80:83], v236, s[4:7], s64 offen offset:2048 nt\n"
  "buffer_load_dwordx4 v[84:87], v236, s[4:7], s64 offen offset:2560 nt\n"
  "buffer_load_dwordx4 v[88:91], v236, s[4:7], s64 offen offset:3072 nt\n"
  "buffer_load_dwordx4 v[92:95], v236, s[4:7], s64 offen offset:3584 nt\n"
  "s_branch L_join_%=\n"
  "L_kinit_%=:\n"
  "s_mov_b64 s[16:17], s[98:99]\n"
  "s_mov_b64 s[20:21], s[60:61]\n"
  "buffer_load_ushort v2, v238, s[8:11], 0 offen\n"
  "buffer_load_ushort v3, v238, s[12:15], 0 offen\n"
  "buffer_load_dwordx2 v[8:9], v237, s[16:19], 0 offen offset:0\n"
  "buffer_load_dwordx2 v[10:11], v237, s[16:19], 0 offen offset:64\n"
  "buffer_load_dwordx2 v[12:13], v237, s[16:19], 0 offen offset:128\n"
  "buffer_load_dwordx2 v[14:15], v237, s[16:19], 0 offen offset:192\n"
  "buffer_load_dwordx2 v[16:17], v237, s[20:23], 0 offen offset:0\n"
  "buffer_load_dwordx2 v[18:19], v237, s[20:23], 0 offen offset:64\n"
  "buffer_load_dwordx2 v[20:21], v237, s[20:23], 0 offen offset:128\n"
  "buffer_load_dwordx2 v[22:23], v237, s[20:23], 0 offen offset:192\n"
  "buffer_load_dwordx4 v[64:67], v236, s[4:7], s64 offen offset:0 nt\n"
  "buffer_load_dwordx4 v[68:71], v236, s[4:7], s64 offen offset:128 nt\n"
  "buffer_load_dwordx4 v[72:75], v236, s[4:7], s64 offen offset:256 nt\n"
  "buffer_load_dwordx4 v[76:79], v236, s[4:7], s64 offen offset:384 nt\n"
  "buffer_load_dwordx4 v[80:83], v236, s[4:7], s65 offen offset:0 nt\n"
  "buffer_load_dwordx4 v[84:87], v236, s[4:7], s65 offen offset:128 nt\n"
  "buffer_load_dwordx4 v[88:91], v236, s[4:7], s65 offen offset:256 nt\n"
  "buffer_load_dwordx4 v[92:95], v236, s[4:7], s65 offen offset:384 nt\n"
  "L_join_%=:\n"
  : "={v[0:3]}"(f0), "={v[8:23]}"(f1), "={v[64:79]}"(f2), "={v[80:95]}"(f3), "={s[4:19]}"(sA), "={s[20:23]}"(sB), "={s[36:39]}"(sW),
    "+{s[80:95]}"(ka0), "+{s[96:99]}"(ka1), "+{s[60:63]}"(ka2), "+{s[28:29]}"(outp), "+{s49}"(w), "+s"(b), "+v"(tid), "+{s[64:67]}"(sraw), "+{v[236:239]}"(pa)
  : "{s77}"(EPOCH_OFF + ((blockIdx.x >> 3) & 1) * 128) : "memory", "scc", "s78");
  unsigned lane = tid & 63;
  const unsigned h = ((b & 7) / GG) * (GG / 2) + (((b >> 3) % GG) >> 1) + 4 * ((b >> 3) & 1), c = ((b & 7) % GG) * (32 / GG) + (b >> 3) / GG;
  const unsigned rho = 16 * (b & 7) + (b >> 4), T = rho >> 1, half = rho & 1, kk = (T >> 2) & 3, vm = 0u - (w >> 1);
  u32x4 sws, sfl;
  #pragma unroll
  for (unsigned e = 0; e < 4; ++e) {
    const unsigned x = sraw[e] + (SEL((w & 1) + 2 * (e & 1), e) << 16);
    sws[e] = x + (16 * (T >> 4) + kk) * KV_TILE_B + SEL(8192, 0);
    sfl[e] = (x >> 8) + FLAG_OFF + ((T >> 4) * 8 + (kk >> 1)) * 128 + ((kk & 1) * 8 + half * 4 + SEL(2, w)) * 8;
  }
  const unsigned j = lane >> 3, il = lane & 7, ldsb = (unsigned)(size_t)lds;
  u32x4 pb;
  pa[3] = (64 * c + 16 * w + j) * 256 + il * 16;
  pb[0] = ldsb + XP_LDS + (w & 1) * 2048 + (il * 8 + ((j + il) & 7)) * 16;
  pb[1] = ldsb + XP_LDS + (w & 1) * 2048 + (j * 8 + ((j + il) & 7)) * 16;
  pb[2] = SEL(((lane >> 4) & 1) * 4096 + (2 * half + (lane >> 5)) * 256 + (lane & 15) * 16, (w & 1) * 4096 + j * 256 + half * 128 + il * 16);
  pb[3] = ldsb + Q_LDS + (w * 4 + (il >> 1)) * 1056 + (il & 1) * 528 + j * 16;
  const unsigned vfs = lane ? 0x80000000u : 0u;
  const unsigned vfs2 = lane == 1 ? sfl[3] : (lane == 0 && w < 2 ? sfl[2] : 0x80000000u);
  const unsigned vfp = ((lane >> 4) & 1) * 128 + ((lane & 12) | ((lane & 3) == 3 ? 2 : (lane & 3))) * 8;
  const unsigned vk = lane * 16, vv = 8192 + lane * 16;
  u32x4 exa, qra;
  #pragma unroll
  for (unsigned x = 0; x < 4; ++x) {
    exa[x] = ldsb + EX_LDS + lane * 16 + (x ^ w ^ 3) * 3072 + x * 1024;
    qra[x] = ldsb + Q_LDS + (x ^ w ^ 3) * 4224 + (lane >> 5) * 528 + (lane & 31) * 16;
  }
  exa[3] = vfp;
  const unsigned vex = ldsb + EX_LDS + w * 3072 + lane * 16;
  const unsigned vet = (lane & 1) * 6176 + (lane & 2) * 25152 + ((lane & 12) + (lane >> 4)) * 16;
  const unsigned vo = (h * S + 64 * c + 16 * w + 4 * (lane >> 4)) * 256 + (lane & 15) * 8;
  const u32x4 cad = {vet + (w ^ 3) * 12544, vet + (w ^ 2) * 12544 + 2048, vet + (w ^ 1) * 12544 + 4096, vex};
  float lo, mo;
  unsigned long long tag;
  unsigned trips, dummy;
  unsigned kvo = (h * NT + w * 16) * KV_TILE_B, flo = FLAG_OFF + (h * 4 + w) * 1024;
  asm volatile(
  "L_begin_%=:\n"
  "s_cmp_ge_u32 s49, 2\n"
  "s_cbranch_scc1 L_vpath_%=\n"
  "s_mov_b64 s[24:25], s[80:81]\n"
  "s_mov_b64 s[16:17], s[94:95]\n"
  "s_mov_b64 s[20:21], s[96:97]\n"
  "s_mov_b32 s26, 4194304\n"
  "s_mov_b32 s27, 0x20000\n"
  "s_mov_b32 s42, 262144\n"
  "s_mov_b32 s43, 0x20000\n"
  "s_mov_b32 s54, 1024\n"
  "s_mov_b32 s55, 0x20000\n"
  "s_add_u32 s40, s40, s62\n"
  "s_addc_u32 s41, s63, 0\n"
  "s_add_u32 s52, s52, s62\n"
  "s_addc_u32 s53, s63, 0\n"
  "v_add_u32_e32 v225, 0x1000, v224\n"
  "v_add_u32_e32 v227, 0x1000, v226\n"
  "s_mov_b32 s47, 0x53800000\n"
  "s_mov_b32 s46, 0x100\n"
  "s_mov_b32 s48, 0x4000\n"
  "s_mov_b32 s44, s56\n"
  "s_xor_b32 s45, s56, 0x4000\n"
  "s_waitcnt vmcnt(18)\n"
  "v_readfirstlane_b32 s78, v0\n"
  "v_readfirstlane_b32 s79, v1\n"
  "s_mul_i32 s57, s79, 0x4C957F2D\n"
  "s_mul_i32 s58, s78, 0x5851F42D\n"
  "s_mul_hi_u32 s51, s78, 0x4C957F2D\n"
  "s_mul_i32 s50, s78, 0x4C957F2D\n"
  "s_add_u32 s51, s51, s57\n"
  "s_add_u32 s51, s51, s58\n"
  "s_add_u32 s50, s50, 0xF767814F\n"
  "s_addc_u32 s51, s51, 0x14057B7E\n"
  "v_mov_b32_e32 v248, s50\n"
  "v_mov_b32_e32 v249, s51\n"
  "s_mov_b32 s57, 0\n"
  "s_lshr_b32 s78, s56, 11\n"
  "s_and_b32 s78, s78, 16\n"
  "s_lshl_b32 s78, 0xffff, s78\n"
  "s_mov_b32 s79, 0\n"
  "s_barrier\n"
  "s_waitcnt vmcnt(16)\n"
  "v_lshlrev_b32_e32 v4, 16, v2\n"
  "v_lshlrev_b32_e32 v5, 16, v3\n"
  "v_mul_f32_e32 v4, 0x3e0293ee, v4\n"
  "v_mul_f32_e64 v5, -v5, v4\n"
  "s_waitcnt vmcnt(8)\n"
  "v_lshlrev_b32_e32 v128, 16, v8\n"
  "v_and_b32_e32 v129, 0xffff0000, v8\n"
  "v_lshlrev_b32_e32 v130, 16, v9\n"
  "v_and_b32_e32 v131, 0xffff0000, v9\n"
  "v_lshlrev_b32_e32 v132, 16, v10\n"
  "v_and_b32_e32 v133, 0xffff0000, v10\n"
  "v_lshlrev_b32_e32 v134, 16, v11\n"
  "v_and_b32_e32 v135, 0xffff0000, v11\n"
  "v_lshlrev_b32_e32 v136, 16, v12\n"
  "v_and_b32_e32 v137, 0xffff0000, v12\n"
  "v_lshlrev_b32_e32 v138, 16, v13\n"
  "v_and_b32_e32 v139, 0xffff0000, v13\n"
  "v_lshlrev_b32_e32 v140, 16, v14\n"
  "v_and_b32_e32 v141, 0xffff0000, v14\n"
  "v_lshlrev_b32_e32 v142, 16, v15\n"
  "v_and_b32_e32 v143, 0xffff0000, v15\n"
  "v_lshlrev_b32_e32 v144, 16, v16\n"
  "v_and_b32_e32 v145, 0xffff0000, v16\n"
  "v_lshlrev_b32_e32 v146, 16, v17\n"
  "v_and_b32_e32 v147, 0xffff0000, v17\n"
  "v_lshlrev_b32_e32 v148, 16, v18\n"
  "v_and_b32_e32 v149, 0xffff0000, v18\n"
  "v_lshlrev_b32_e32 v150, 16, v19\n"
  "v_and_b32_e32 v151, 0xffff0000, v19\n"
  "v_lshlrev_b32_e32 v152, 16, v20\n"
  "v_and_b32_e32 v153, 0xffff0000, v20\n"
  "v_lshlrev_b32_e32 v154, 16, v21\n"
  "v_and_b32_e32 v155, 0xffff0000, v21\n"
  "v_lshlrev_b32_e32 v156, 16, v22\n"
  "v_and_b32_e32 v157, 0xffff0000, v22\n"
  "v_lshlrev_b32_e32 v158, 16, v23\n"
  "v_and_b32_e32 v159, 0xffff0000, v23\n"
  "v_mov_b32_dpp v6, v4 quad_perm:[0,0,0,0] row_mask:0xf bank_mask:0xf\n"
  "v_mov_b32_dpp v7, v5 quad_perm:[0,0,0,0] row_mask:0xf bank_mask:0xf\n"
  "s_waitcnt vmcnt(4)\n"
  "v_cvt_f32_i32_e32 v64, v64\n"
  "v_cvt_f32_i32_e32 v65, v65\n"
  "v_cvt_f32_i32_e32 v66, v66\n"
  "v_cvt_f32_i32_e32 v67, v67\n"
  "v_cvt_f32_i32_e32 v72, v72\n"
  "v_cvt_f32_i32_e32 v73, v73\n"
  "v_cvt_f32_i32_e32 v74, v74\n"
  "v_cvt_f32_i32_e32 v75, v75\n"
  "v_pk_fma_f32 v[64:65], v[64:65], v[6:7], v[6:7] op_sel:[0,0,1] op_sel_hi:[1,0,1]\n"
  "v_pk_fma_f32 v[66:67], v[66:67], v[6:7], v[6:7] op_sel:[0,0,1] op_sel_hi:[1,0,1]\n"
  "v_pk_fma_f32 v[72:73], v[72:73], v[6:7], v[6:7] op_sel:[0,0,1] op_sel_hi:[1,0,1]\n"
  "v_pk_fma_f32 v[74:75], v[74:75], v[6:7], v[6:7] op_sel:[0,0,1] op_sel_hi:[1,0,1]\n"
  "v_pk_mul_f32 v[44:45], v[64:65], v[152:153]\n"
  "v_pk_mul_f32 v[40:41], v[72:73], v[144:145]\n"
  "v_pk_mul_f32 v[46:47], v[66:67], v[154:155]\n"
  "v_pk_mul_f32 v[42:43], v[74:75], v[146:147]\n"
  "v_pk_fma_f32 v[40:41], v[64:65], v[128:129], v[40:41] neg_lo:[0,0,1] neg_hi:[0,0,1]\n"
  "v_pk_fma_f32 v[44:45], v[72:73], v[136:137], v[44:45]\n"
  "v_pk_fma_f32 v[42:43], v[66:67], v[130:131], v[42:43] neg_lo:[0,0,1] neg_hi:[0,0,1]\n"
  "v_pk_fma_f32 v[46:47], v[74:75], v[138:139], v[46:47]\n"
  "v_cvt_pk_bf16_f32 v48, v40, v41\n"
  "v_cvt_pk_bf16_f32 v49, v42, v43\n"
  "v_cvt_pk_bf16_f32 v50, v44, v45\n"
  "v_cvt_pk_bf16_f32 v51, v46, v47\n"
  "ds_write_b128 v240, v[48:51] offset:0\n"
  "v_cvt_f32_i32_e32 v68, v68\n"
  "v_cvt_f32_i32_e32 v69, v69\n"
  "v_cvt_f32_i32_e32 v70, v70\n"
  "v_cvt_f32_i32_e32 v71, v71\n"
  "v_cvt_f32_i32_e32 v76, v76\n"
  "v_cvt_f32_i32_e32 v77, v77\n"
  "v_cvt_f32_i32_e32 v78, v78\n"
  "v_cvt_f32_i32_e32 v79, v79\n"
  "v_pk_fma_f32 v[68:69], v[68:69], v[6:7], v[6:7] op_sel:[0,0,1] op_sel_hi:[1,0,1]\n"
  "v_pk_fma_f32 v[70:71], v[70:71], v[6:7], v[6:7] op_sel:[0,0,1] op_sel_hi:[1,0,1]\n"
  "v_pk_fma_f32 v[76:77], v[76:77], v[6:7], v[6:7] op_sel:[0,0,1] op_sel_hi:[1,0,1]\n"
  "v_pk_fma_f32 v[78:79], v[78:79], v[6:7], v[6:7] op_sel:[0,0,1] op_sel_hi:[1,0,1]\n"
  "v_pk_mul_f32 v[44:45], v[68:69], v[156:157]\n"
  "v_pk_mul_f32 v[40:41], v[76:77], v[148:149]\n"
  "v_pk_mul_f32 v[46:47], v[70:71], v[158:159]\n"
  "v_pk_mul_f32 v[42:43], v[78:79], v[150:151]\n"
  "v_pk_fma_f32 v[40:41], v[68:69], v[132:133], v[40:41] neg_lo:[0,0,1] neg_hi:[0,0,1]\n"
  "v_pk_fma_f32 v[44:45], v[76:77], v[140:141], v[44:45]\n"
  "v_pk_fma_f32 v[42:43], v[70:71], v[134:135], v[42:43] neg_lo:[0,0,1] neg_hi:[0,0,1]\n"
  "v_pk_fma_f32 v[46:47], v[78:79], v[142:143], v[46:47]\n"
  "v_cvt_pk_bf16_f32 v52, v40, v41\n"
  "v_cvt_pk_bf16_f32 v53, v42, v43\n"
  "v_cvt_pk_bf16_f32 v54, v44, v45\n"
  "v_cvt_pk_bf16_f32 v55, v46, v47\n"
  "ds_write_b128 v240, v[52:55] offset:1024\n"
  "ds_read_b128 v[56:59], v241\n"
  "ds_read_b128 v[60:63], v241 offset:1024\n"
  "s_waitcnt lgkmcnt(0)\n"
  "buffer_store_dwordx4 v[56:59], v242, s[36:39], s68 offen sc1\n"
  "buffer_store_dwordx4 v[60:63], v242, s[36:39], s68 offen offset:2048 sc1\n"
  "buffer_load_dwordx4 v[160:163], v239, s[24:27], s76 offen offset:0 nt\n"
  "buffer_load_dwordx4 v[164:167], v239, s[24:27], s76 offen offset:128 nt\n"
  "buffer_load_dwordx4 v[168:171], v239, s[16:19], 0 offen offset:0\n"
  "buffer_load_dwordx4 v[172:175], v239, s[16:19], 0 offen offset:128\n"
  "buffer_load_dwordx4 v[176:179], v239, s[20:23], 0 offen offset:0\n"
  "buffer_load_dwordx4 v[180:183], v239, s[20:23], 0 offen offset:128\n"
  "v_mov_b32_dpp v6, v4 quad_perm:[1,1,1,1] row_mask:0xf bank_mask:0xf\n"
  "v_mov_b32_dpp v7, v5 quad_perm:[1,1,1,1] row_mask:0xf bank_mask:0xf\n"
  "s_waitcnt vmcnt(8)\n"
  "v_cvt_f32_i32_e32 v80, v80\n"
  "v_cvt_f32_i32_e32 v81, v81\n"
  "v_cvt_f32_i32_e32 v82, v82\n"
  "v_cvt_f32_i32_e32 v83, v83\n"
  "v_cvt_f32_i32_e32 v88, v88\n"
  "v_cvt_f32_i32_e32 v89, v89\n"
  "v_cvt_f32_i32_e32 v90, v90\n"
  "v_cvt_f32_i32_e32 v91, v91\n"
  "v_pk_fma_f32 v[80:81], v[80:81], v[6:7], v[6:7] op_sel:[0,0,1] op_sel_hi:[1,0,1]\n"
  "v_pk_fma_f32 v[82:83], v[82:83], v[6:7], v[6:7] op_sel:[0,0,1] op_sel_hi:[1,0,1]\n"
  "v_pk_fma_f32 v[88:89], v[88:89], v[6:7], v[6:7] op_sel:[0,0,1] op_sel_hi:[1,0,1]\n"
  "v_pk_fma_f32 v[90:91], v[90:91], v[6:7], v[6:7] op_sel:[0,0,1] op_sel_hi:[1,0,1]\n"
  "v_pk_mul_f32 v[44:45], v[80:81], v[152:153]\n"
  "v_pk_mul_f32 v[40:41], v[88:89], v[144:145]\n"
  "v_pk_mul_f32 v[46:47], v[82:83], v[154:155]\n"
  "v_pk_mul_f32 v[42:43], v[90:91], v[146:147]\n"
  "v_pk_fma_f32 v[40:41], v[80:81], v[128:129], v[40:41] neg_lo:[0,0,1] neg_hi:[0,0,1]\n"
  "v_pk_fma_f32 v[44:45], v[88:89], v[136:137], v[44:45]\n"
  "v_pk_fma_f32 v[42:43], v[82:83], v[130:131], v[42:43] neg_lo:[0,0,1] neg_hi:[0,0,1]\n"
  "v_pk_fma_f32 v[46:47], v[90:91], v[138:139], v[46:47]\n"
  "v_cvt_pk_bf16_f32 v48, v40, v41\n"
  "v_cvt_pk_bf16_f32 v49, v42, v43\n"
  "v_cvt_pk_bf16_f32 v50, v44, v45\n"
  "v_cvt_pk_bf16_f32 v51, v46, v47\n"
  "ds_write_b128 v240, v[48:51] offset:0\n"
  "v_cvt_f32_i32_e32 v84, v84\n"
  "v_cvt_f32_i32_e32 v85, v85\n"
  "v_cvt_f32_i32_e32 v86, v86\n"
  "v_cvt_f32_i32_e32 v87, v87\n"
  "v_cvt_f32_i32_e32 v92, v92\n"
  "v_cvt_f32_i32_e32 v93, v93\n"
  "v_cvt_f32_i32_e32 v94, v94\n"
  "v_cvt_f32_i32_e32 v95, v95\n"
  "v_pk_fma_f32 v[84:85], v[84:85], v[6:7], v[6:7] op_sel:[0,0,1] op_sel_hi:[1,0,1]\n"
  "v_pk_fma_f32 v[86:87], v[86:87], v[6:7], v[6:7] op_sel:[0,0,1] op_sel_hi:[1,0,1]\n"
  "v_pk_fma_f32 v[92:93], v[92:93], v[6:7], v[6:7] op_sel:[0,0,1] op_sel_hi:[1,0,1]\n"
  "v_pk_fma_f32 v[94:95], v[94:95], v[6:7], v[6:7] op_sel:[0,0,1] op_sel_hi:[1,0,1]\n"
  "v_pk_mul_f32 v[44:45], v[84:85], v[156:157]\n"
  "v_pk_mul_f32 v[40:41], v[92:93], v[148:149]\n"
  "v_pk_mul_f32 v[46:47], v[86:87], v[158:159]\n"
  "v_pk_mul_f32 v[42:43], v[94:95], v[150:151]\n"
  "v_pk_fma_f32 v[40:41], v[84:85], v[132:133], v[40:41] neg_lo:[0,0,1] neg_hi:[0,0,1]\n"
  "v_pk_fma_f32 v[44:45], v[92:93], v[140:141], v[44:45]\n"
  "v_pk_fma_f32 v[42:43], v[86:87], v[134:135], v[42:43] neg_lo:[0,0,1] neg_hi:[0,0,1]\n"
  "v_pk_fma_f32 v[46:47], v[94:95], v[142:143], v[46:47]\n"
  "v_cvt_pk_bf16_f32 v52, v40, v41\n"
  "v_cvt_pk_bf16_f32 v53, v42, v43\n"
  "v_cvt_pk_bf16_f32 v54, v44, v45\n"
  "v_cvt_pk_bf16_f32 v55, v46, v47\n"
  "ds_write_b128 v240, v[52:55] offset:1024\n"
  "ds_read_b128 v[56:59], v241\n"
  "ds_read_b128 v[60:63], v241 offset:1024\n"
  "s_waitcnt lgkmcnt(0)\n"
  "buffer_store_dwordx4 v[56:59], v242, s[36:39], s69 offen sc1\n"
  "buffer_store_dwordx4 v[60:63], v242, s[36:39], s69 offen offset:2048 sc1\n"
  "s_waitcnt vmcnt(8)\n"
  "buffer_store_dwordx2 v[248:249], v244, s[36:39], s72 offen sc1\n"
  "buffer_load_dwordx4 v[184:187], v239, s[24:27], s76 offen offset:2048 nt\n"
  "buffer_load_dwordx4 v[188:191], v239, s[24:27], s76 offen offset:2176 nt\n"
  "buffer_load_dwordx4 v[192:195], v239, s[16:19], 0 offen offset:2048\n"
  "buffer_load_dwordx4 v[196:199], v239, s[16:19], 0 offen offset:2176\n"
  "buffer_load_dwordx4 v[200:203], v239, s[20:23], 0 offen offset:2048\n"
  "buffer_load_dwordx4 v[204:207], v239, s[20:23], 0 offen offset:2176\n"
  "s_waitcnt vmcnt(9)\n"
  "v_lshlrev_b32_e32 v208, 16, v160\n"
  "v_and_b32_e32 v209, 0xffff0000, v160\n"
  "v_lshlrev_b32_e32 v210, 16, v164\n"
  "v_and_b32_e32 v211, 0xffff0000, v164\n"
  "v_lshlrev_b32_e32 v216, 16, v176\n"
  "v_and_b32_e32 v217, 0xffff0000, v176\n"
  "v_lshlrev_b32_e32 v218, 16, v180\n"
  "v_and_b32_e32 v219, 0xffff0000, v180\n"
  "v_lshlrev_b32_e32 v212, 16, v168\n"
  "v_and_b32_e32 v213, 0xffff0000, v168\n"
  "v_lshlrev_b32_e32 v214, 16, v172\n"
  "v_and_b32_e32 v215, 0xffff0000, v172\n"
  "v_pk_mul_f32 v[216:217], v[210:211], v[216:217]\n"
  "v_pk_mul_f32 v[218:219], v[208:209], v[218:219]\n"
  "v_pk_fma_f32 v[216:217], v[208:209], v[212:213], v[216:217] neg_lo:[0,0,1] neg_hi:[0,0,1]\n"
  "v_pk_fma_f32 v[218:219], v[210:211], v[214:215], v[218:219]\n"
  "v_cvt_pk_bf16_f32 v112, v216, v217\n"
  "v_cvt_pk_bf16_f32 v114, v218, v219\n"
  "v_lshlrev_b32_e32 v208, 16, v161\n"
  "v_and_b32_e32 v209, 0xffff0000, v161\n"
  "v_lshlrev_b32_e32 v210, 16, v165\n"
  "v_and_b32_e32 v211, 0xffff0000, v165\n"
  "v_lshlrev_b32_e32 v216, 16, v177\n"
  "v_and_b32_e32 v217, 0xffff0000, v177\n"
  "v_lshlrev_b32_e32 v218, 16, v181\n"
  "v_and_b32_e32 v219, 0xffff0000, v181\n"
  "v_lshlrev_b32_e32 v212, 16, v169\n"
  "v_and_b32_e32 v213, 0xffff0000, v169\n"
  "v_lshlrev_b32_e32 v214, 16, v173\n"
  "v_and_b32_e32 v215, 0xffff0000, v173\n"
  "v_pk_mul_f32 v[216:217], v[210:211], v[216:217]\n"
  "v_pk_mul_f32 v[218:219], v[208:209], v[218:219]\n"
  "v_pk_fma_f32 v[216:217], v[208:209], v[212:213], v[216:217] neg_lo:[0,0,1] neg_hi:[0,0,1]\n"
  "v_pk_fma_f32 v[218:219], v[210:211], v[214:215], v[218:219]\n"
  "v_cvt_pk_bf16_f32 v113, v216, v217\n"
  "v_cvt_pk_bf16_f32 v115, v218, v219\n"
  "v_lshlrev_b32_e32 v208, 16, v162\n"
  "v_and_b32_e32 v209, 0xffff0000, v162\n"
  "v_lshlrev_b32_e32 v210, 16, v166\n"
  "v_and_b32_e32 v211, 0xffff0000, v166\n"
  "v_lshlrev_b32_e32 v216, 16, v178\n"
  "v_and_b32_e32 v217, 0xffff0000, v178\n"
  "v_lshlrev_b32_e32 v218, 16, v182\n"
  "v_and_b32_e32 v219, 0xffff0000, v182\n"
  "v_lshlrev_b32_e32 v212, 16, v170\n"
  "v_and_b32_e32 v213, 0xffff0000, v170\n"
  "v_lshlrev_b32_e32 v214, 16, v174\n"
  "v_and_b32_e32 v215, 0xffff0000, v174\n"
  "v_pk_mul_f32 v[216:217], v[210:211], v[216:217]\n"
  "v_pk_mul_f32 v[218:219], v[208:209], v[218:219]\n"
  "v_pk_fma_f32 v[216:217], v[208:209], v[212:213], v[216:217] neg_lo:[0,0,1] neg_hi:[0,0,1]\n"
  "v_pk_fma_f32 v[218:219], v[210:211], v[214:215], v[218:219]\n"
  "v_cvt_pk_bf16_f32 v116, v216, v217\n"
  "v_cvt_pk_bf16_f32 v118, v218, v219\n"
  "v_lshlrev_b32_e32 v208, 16, v163\n"
  "v_and_b32_e32 v209, 0xffff0000, v163\n"
  "v_lshlrev_b32_e32 v210, 16, v167\n"
  "v_and_b32_e32 v211, 0xffff0000, v167\n"
  "v_lshlrev_b32_e32 v216, 16, v179\n"
  "v_and_b32_e32 v217, 0xffff0000, v179\n"
  "v_lshlrev_b32_e32 v218, 16, v183\n"
  "v_and_b32_e32 v219, 0xffff0000, v183\n"
  "v_lshlrev_b32_e32 v212, 16, v171\n"
  "v_and_b32_e32 v213, 0xffff0000, v171\n"
  "v_lshlrev_b32_e32 v214, 16, v175\n"
  "v_and_b32_e32 v215, 0xffff0000, v175\n"
  "v_pk_mul_f32 v[216:217], v[210:211], v[216:217]\n"
  "v_pk_mul_f32 v[218:219], v[208:209], v[218:219]\n"
  "v_pk_fma_f32 v[216:217], v[208:209], v[212:213], v[216:217] neg_lo:[0,0,1] neg_hi:[0,0,1]\n"
  "v_pk_fma_f32 v[218:219], v[210:211], v[214:215], v[218:219]\n"
  "v_cvt_pk_bf16_f32 v117, v216, v217\n"
  "v_cvt_pk_bf16_f32 v119, v218, v219\n"
  "ds_write_b128 v243, v[112:115] offset:0\n"
  "ds_write_b128 v243, v[116:119] offset:256\n"
  "buffer_load_dwordx4 v[96:99], v236, s[4:7], s66 offen offset:0 nt\n"
  "buffer_load_dwordx4 v[100:103], v236, s[4:7], s66 offen offset:128 nt\n"
  "buffer_load_dwordx4 v[104:107], v236, s[4:7], s66 offen offset:256 nt\n"
  "buffer_load_dwordx4 v[108:111], v236, s[4:7], s66 offen offset:384 nt\n"
  "s_waitcnt vmcnt(11)\n"
  "buffer_store_dwordx2 v[248:249], v244, s[36:39], s73 offen sc1\n"
  "s_waitcnt vmcnt(5)\n"
  "v_lshlrev_b32_e32 v208, 16, v184\n"
  "v_and_b32_e32 v209, 0xffff0000, v184\n"
  "v_lshlrev_b32_e32 v210, 16, v188\n"
  "v_and_b32_e32 v211, 0xffff0000, v188\n"
  "v_lshlrev_b32_e32 v216, 16, v200\n"
  "v_and_b32_e32 v217, 0xffff0000, v200\n"
  "v_lshlrev_b32_e32 v218, 16, v204\n"
  "v_and_b32_e32 v219, 0xffff0000, v204\n"
  "v_lshlrev_b32_e32 v212, 16, v192\n"
  "v_and_b32_e32 v213, 0xffff0000, v192\n"
  "v_lshlrev_b32_e32 v214, 16, v196\n"
  "v_and_b32_e32 v215, 0xffff0000, v196\n"
  "v_pk_mul_f32 v[216:217], v[210:211], v[216:217]\n"
  "v_pk_mul_f32 v[218:219], v[208:209], v[218:219]\n"
  "v_pk_fma_f32 v[216:217], v[208:209], v[212:213], v[216:217] neg_lo:[0,0,1] neg_hi:[0,0,1]\n"
  "v_pk_fma_f32 v[218:219], v[210:211], v[214:215], v[218:219]\n"
  "v_cvt_pk_bf16_f32 v120, v216, v217\n"
  "v_cvt_pk_bf16_f32 v122, v218, v219\n"
  "v_lshlrev_b32_e32 v208, 16, v185\n"
  "v_and_b32_e32 v209, 0xffff0000, v185\n"
  "v_lshlrev_b32_e32 v210, 16, v189\n"
  "v_and_b32_e32 v211, 0xffff0000, v189\n"
  "v_lshlrev_b32_e32 v216, 16, v201\n"
  "v_and_b32_e32 v217, 0xffff0000, v201\n"
  "v_lshlrev_b32_e32 v218, 16, v205\n"
  "v_and_b32_e32 v219, 0xffff0000, v205\n"
  "v_lshlrev_b32_e32 v212, 16, v193\n"
  "v_and_b32_e32 v213, 0xffff0000, v193\n"
  "v_lshlrev_b32_e32 v214, 16, v197\n"
  "v_and_b32_e32 v215, 0xffff0000, v197\n"
  "v_pk_mul_f32 v[216:217], v[210:211], v[216:217]\n"
  "v_pk_mul_f32 v[218:219], v[208:209], v[218:219]\n"
  "v_pk_fma_f32 v[216:217], v[208:209], v[212:213], v[216:217] neg_lo:[0,0,1] neg_hi:[0,0,1]\n"
  "v_pk_fma_f32 v[218:219], v[210:211], v[214:215], v[218:219]\n"
  "v_cvt_pk_bf16_f32 v121, v216, v217\n"
  "v_cvt_pk_bf16_f32 v123, v218, v219\n"
  "v_lshlrev_b32_e32 v208, 16, v186\n"
  "v_and_b32_e32 v209, 0xffff0000, v186\n"
  "v_lshlrev_b32_e32 v210, 16, v190\n"
  "v_and_b32_e32 v211, 0xffff0000, v190\n"
  "v_lshlrev_b32_e32 v216, 16, v202\n"
  "v_and_b32_e32 v217, 0xffff0000, v202\n"
  "v_lshlrev_b32_e32 v218, 16, v206\n"
  "v_and_b32_e32 v219, 0xffff0000, v206\n"
  "v_lshlrev_b32_e32 v212, 16, v194\n"
  "v_and_b32_e32 v213, 0xffff0000, v194\n"
  "v_lshlrev_b32_e32 v214, 16, v198\n"
  "v_and_b32_e32 v215, 0xffff0000, v198\n"
  "v_pk_mul_f32 v[216:217], v[210:211], v[216:217]\n"
  "v_pk_mul_f32 v[218:219], v[208:209], v[218:219]\n"
  "v_pk_fma_f32 v[216:217], v[208:209], v[212:213], v[216:217] neg_lo:[0,0,1] neg_hi:[0,0,1]\n"
  "v_pk_fma_f32 v[218:219], v[210:211], v[214:215], v[218:219]\n"
  "v_cvt_pk_bf16_f32 v124, v216, v217\n"
  "v_cvt_pk_bf16_f32 v126, v218, v219\n"
  "v_lshlrev_b32_e32 v208, 16, v187\n"
  "v_and_b32_e32 v209, 0xffff0000, v187\n"
  "v_lshlrev_b32_e32 v210, 16, v191\n"
  "v_and_b32_e32 v211, 0xffff0000, v191\n"
  "v_lshlrev_b32_e32 v216, 16, v203\n"
  "v_and_b32_e32 v217, 0xffff0000, v203\n"
  "v_lshlrev_b32_e32 v218, 16, v207\n"
  "v_and_b32_e32 v219, 0xffff0000, v207\n"
  "v_lshlrev_b32_e32 v212, 16, v195\n"
  "v_and_b32_e32 v213, 0xffff0000, v195\n"
  "v_lshlrev_b32_e32 v214, 16, v199\n"
  "v_and_b32_e32 v215, 0xffff0000, v199\n"
  "v_pk_mul_f32 v[216:217], v[210:211], v[216:217]\n"
  "v_pk_mul_f32 v[218:219], v[208:209], v[218:219]\n"
  "v_pk_fma_f32 v[216:217], v[208:209], v[212:213], v[216:217] neg_lo:[0,0,1] neg_hi:[0,0,1]\n"
  "v_pk_fma_f32 v[218:219], v[210:211], v[214:215], v[218:219]\n"
  "v_cvt_pk_bf16_f32 v125, v216, v217\n"
  "v_cvt_pk_bf16_f32 v127, v218, v219\n"
  "ds_write_b128 v243, v[120:123] offset:128\n"
  "ds_write_b128 v243, v[124:127] offset:384\n"
  "buffer_load_dwordx2 v[222:223], v235, s[52:55], s57 offen sc1\n"
  "buffer_load_dwordx4 v[24:27], v236, s[4:7], s67 offen offset:0 nt\n"
  "buffer_load_dwordx4 v[28:31], v236, s[4:7], s67 offen offset:128 nt\n"
  "buffer_load_dwordx4 v[32:35], v236, s[4:7], s67 offen offset:256 nt\n"
  "buffer_load_dwordx4 v[36:39], v236, s[4:7], s67 offen offset:384 nt\n"
  "v_mov_b32_dpp v6, v4 quad_perm:[2,2,2,2] row_mask:0xf bank_mask:0xf\n"
  "v_mov_b32_dpp v7, v5 quad_perm:[2,2,2,2] row_mask:0xf bank_mask:0xf\n"
  "s_waitcnt vmcnt(6)\n"
  "v_cvt_f32_i32_e32 v96, v96\n"
  "v_cvt_f32_i32_e32 v97, v97\n"
  "v_cvt_f32_i32_e32 v98, v98\n"
  "v_cvt_f32_i32_e32 v99, v99\n"
  "v_cvt_f32_i32_e32 v104, v104\n"
  "v_cvt_f32_i32_e32 v105, v105\n"
  "v_cvt_f32_i32_e32 v106, v106\n"
  "v_cvt_f32_i32_e32 v107, v107\n"
  "v_pk_fma_f32 v[96:97], v[96:97], v[6:7], v[6:7] op_sel:[0,0,1] op_sel_hi:[1,0,1]\n"
  "v_pk_fma_f32 v[98:99], v[98:99], v[6:7], v[6:7] op_sel:[0,0,1] op_sel_hi:[1,0,1]\n"
  "v_pk_fma_f32 v[104:105], v[104:105], v[6:7], v[6:7] op_sel:[0,0,1] op_sel_hi:[1,0,1]\n"
  "v_pk_fma_f32 v[106:107], v[106:107], v[6:7], v[6:7] op_sel:[0,0,1] op_sel_hi:[1,0,1]\n"
  "v_pk_mul_f32 v[44:45], v[96:97], v[152:153]\n"
  "v_pk_mul_f32 v[40:41], v[104:105], v[144:145]\n"
  "v_pk_mul_f32 v[46:47], v[98:99], v[154:155]\n"
  "v_pk_mul_f32 v[42:43], v[106:107], v[146:147]\n"
  "v_pk_fma_f32 v[40:41], v[96:97], v[128:129], v[40:41] neg_lo:[0,0,1] neg_hi:[0,0,1]\n"
  "v_pk_fma_f32 v[44:45], v[104:105], v[136:137], v[44:45]\n"
  "v_pk_fma_f32 v[42:43], v[98:99], v[130:131], v[42:43] neg_lo:[0,0,1] neg_hi:[0,0,1]\n"
  "v_pk_fma_f32 v[46:47], v[106:107], v[138:139], v[46:47]\n"
  "v_cvt_pk_bf16_f32 v48, v40, v41\n"
  "v_cvt_pk_bf16_f32 v49, v42, v43\n"
  "v_cvt_pk_bf16_f32 v50, v44, v45\n"
  "v_cvt_pk_bf16_f32 v51, v46, v47\n"
  "ds_write_b128 v240, v[48:51] offset:0\n"
  "v_cvt_f32_i32_e32 v100, v100\n"
  "v_cvt_f32_i32_e32 v101, v101\n"
  "v_cvt_f32_i32_e32 v102, v102\n"
  "v_cvt_f32_i32_e32 v103, v103\n"
  "v_cvt_f32_i32_e32 v108, v108\n"
  "v_cvt_f32_i32_e32 v109, v109\n"
  "v_cvt_f32_i32_e32 v110, v110\n"
  "v_cvt_f32_i32_e32 v111, v111\n"
  "v_pk_fma_f32 v[100:101], v[100:101], v[6:7], v[6:7] op_sel:[0,0,1] op_sel_hi:[1,0,1]\n"
  "v_pk_fma_f32 v[102:103], v[102:103], v[6:7], v[6:7] op_sel:[0,0,1] op_sel_hi:[1,0,1]\n"
  "v_pk_fma_f32 v[108:109], v[108:109], v[6:7], v[6:7] op_sel:[0,0,1] op_sel_hi:[1,0,1]\n"
  "v_pk_fma_f32 v[110:111], v[110:111], v[6:7], v[6:7] op_sel:[0,0,1] op_sel_hi:[1,0,1]\n"
  "v_pk_mul_f32 v[44:45], v[100:101], v[156:157]\n"
  "v_pk_mul_f32 v[40:41], v[108:109], v[148:149]\n"
  "v_pk_mul_f32 v[46:47], v[102:103], v[158:159]\n"
  "v_pk_mul_f32 v[42:43], v[110:111], v[150:151]\n"
  "v_pk_fma_f32 v[40:41], v[100:101], v[132:133], v[40:41] neg_lo:[0,0,1] neg_hi:[0,0,1]\n"
  "v_pk_fma_f32 v[44:45], v[108:109], v[140:141], v[44:45]\n"
  "v_pk_fma_f32 v[42:43], v[102:103], v[134:135], v[42:43] neg_lo:[0,0,1] neg_hi:[0,0,1]\n"
  "v_pk_fma_f32 v[46:47], v[110:111], v[142:143], v[46:47]\n"
  "v_cvt_pk_bf16_f32 v52, v40, v41\n"
  "v_cvt_pk_bf16_f32 v53, v42, v43\n"
  "v_cvt_pk_bf16_f32 v54, v44, v45\n"
  "v_cvt_pk_bf16_f32 v55, v46, v47\n"
  "ds_write_b128 v240, v[52:55] offset:1024\n"
  "ds_read_b128 v[16:19], v241\n"
  "ds_read_b128 v[20:23], v241 offset:1024\n"
  "v_add_u32_e32 v3, s70, v242\n"
  "s_waitcnt vmcnt(4)\n"
  "v_cmp_ne_u64_e32 vcc, s[50:51], v[222:223]\n"
  "s_and_b64 vcc, vcc, s[78:79]\n"
  "s_cselect_b32 s58, 0x7ff00000, s44\n"
  "buffer_load_dwordx4 a[192:195], v224, s[40:43], s58 offen offset:0 sc1\n"
  "buffer_load_dwordx4 a[196:199], v224, s[40:43], s58 offen offset:1024 sc1\n"
  "buffer_load_dwordx4 a[200:203], v224, s[40:43], s58 offen offset:2048 sc1\n"
  "buffer_load_dwordx4 a[204:207], v224, s[40:43], s58 offen offset:3072 sc1\n"
  "buffer_load_dwordx4 a[208:211], v225, s[40:43], s58 offen offset:0 sc1\n"
  "buffer_load_dwordx4 a[212:215], v225, s[40:43], s58 offen offset:1024 sc1\n"
  "buffer_load_dwordx4 a[216:219], v225, s[40:43], s58 offen offset:2048 sc1\n"
  "buffer_load_dwordx4 a[220:223], v225, s[40:43], s58 offen offset:3072 sc1\n"
  "buffer_load_dwordx4 a[96:99], v226, s[40:43], s58 offen offset:0 sc1\n"
  "buffer_load_dwordx4 a[100:103], v226, s[40:43], s58 offen offset:1024 sc1\n"
  "buffer_load_dwordx4 a[104:107], v226, s[40:43], s58 offen offset:2048 sc1\n"
  "buffer_load_dwordx4 a[108:111], v226, s[40:43], s58 offen offset:3072 sc1\n"
  "buffer_load_dwordx4 a[112:115], v227, s[40:43], s58 offen offset:0 sc1\n"
  "buffer_load_dwordx4 a[116:119], v227, s[40:43], s58 offen offset:1024 sc1\n"
  "buffer_load_dwordx4 a[120:123], v227, s[40:43], s58 offen offset:2048 sc1\n"
  "buffer_load_dwordx4 a[124:127], v227, s[40:43], s58 offen offset:3072 sc1\n"
  "buffer_load_dwordx2 v[222:223], v235, s[52:55], s57 offen sc1\n"
  "s_waitcnt lgkmcnt(0)\n"
  "s_barrier\n"
  "ds_read_b128 a[128:131], v228 offset:0\n"
  "ds_read_b128 a[132:135], v228 offset:1056\n"
  "ds_read_b128 a[136:139], v228 offset:2112\n"
  "ds_read_b128 a[140:143], v228 offset:3168\n"
  "ds_read_b128 a[144:147], v229 offset:0\n"
  "ds_read_b128 a[148:151], v229 offset:1056\n"
  "ds_read_b128 a[152:155], v229 offset:2112\n"
  "ds_read_b128 a[156:159], v229 offset:3168\n"
  "ds_read_b128 a[160:163], v230 offset:0\n"
  "ds_read_b128 a[164:167], v230 offset:1056\n"
  "ds_read_b128 a[168:171], v230 offset:2112\n"
  "ds_read_b128 a[172:175], v230 offset:3168\n"
  "ds_read_b128 a[176:179], v231 offset:0\n"
  "ds_read_b128 a[180:183], v231 offset:1056\n"
  "ds_read_b128 a[184:187], v231 offset:2112\n"
  "ds_read_b128 a[188:191], v231 offset:3168\n"
  "v_mov_b32_e32 v192, 0\n"
  "v_mov_b32_e32 v193, 0\n"
  "v_mov_b32_e32 v194, 0\n"
  "v_mov_b32_e32 v195, 0\n"
  "v_mov_b32_e32 v200, 0\n"
  "v_mov_b32_e32 v201, 0\n"
  "v_mov_b32_e32 v202, 0\n"
  "v_mov_b32_e32 v203, 0\n"
  "v_mov_b32_e32 v208, v246\n"
  "v_mov_b32_e32 v209, v246\n"
  "v_mov_b32_e32 v210, v246\n"
  "v_mov_b32_e32 v211, v246\n"
  "v_mov_b32_e32 v212, 0xff800000\n"
  "v_mov_b32_e32 v213, 0xff800000\n"
  "v_mov_b32_e32 v214, 0xff800000\n"
  "v_mov_b32_e32 v215, 0xff800000\n"
  "v_mov_b32_dpp v6, v4 quad_perm:[3,3,3,3] row_mask:0xf bank_mask:0xf\n"
  "v_mov_b32_dpp v7, v5 quad_perm:[3,3,3,3] row_mask:0xf bank_mask:0xf\n"
  "s_waitcnt vmcnt(17)\n"
  "v_cvt_f32_i32_e32 v24, v24\n"
  "v_cvt_f32_i32_e32 v25, v25\n"
  "v_cvt_f32_i32_e32 v26, v26\n"
  "v_cvt_f32_i32_e32 v27, v27\n"
  "v_cvt_f32_i32_e32 v32, v32\n"
  "v_cvt_f32_i32_e32 v33, v33\n"
  "v_cvt_f32_i32_e32 v34, v34\n"
  "v_cvt_f32_i32_e32 v35, v35\n"
  "v_pk_fma_f32 v[24:25], v[24:25], v[6:7], v[6:7] op_sel:[0,0,1] op_sel_hi:[1,0,1]\n"
  "v_pk_fma_f32 v[26:27], v[26:27], v[6:7], v[6:7] op_sel:[0,0,1] op_sel_hi:[1,0,1]\n"
  "v_pk_fma_f32 v[32:33], v[32:33], v[6:7], v[6:7] op_sel:[0,0,1] op_sel_hi:[1,0,1]\n"
  "v_pk_fma_f32 v[34:35], v[34:35], v[6:7], v[6:7] op_sel:[0,0,1] op_sel_hi:[1,0,1]\n"
  "v_pk_mul_f32 v[44:45], v[24:25], v[152:153]\n"
  "v_pk_mul_f32 v[40:41], v[32:33], v[144:145]\n"
  "v_pk_mul_f32 v[46:47], v[26:27], v[154:155]\n"
  "v_pk_mul_f32 v[42:43], v[34:35], v[146:147]\n"
  "v_pk_fma_f32 v[40:41], v[24:25], v[128:129], v[40:41] neg_lo:[0,0,1] neg_hi:[0,0,1]\n"
  "v_pk_fma_f32 v[44:45], v[32:33], v[136:137], v[44:45]\n"
  "v_pk_fma_f32 v[42:43], v[26:27], v[130:131], v[42:43] neg_lo:[0,0,1] neg_hi:[0,0,1]\n"
  "v_pk_fma_f32 v[46:47], v[34:35], v[138:139], v[46:47]\n"
  "v_cvt_pk_bf16_f32 v48, v40, v41\n"
  "v_cvt_pk_bf16_f32 v49, v42, v43\n"
  "v_cvt_pk_bf16_f32 v50, v44, v45\n"
  "v_cvt_pk_bf16_f32 v51, v46, v47\n"
  "ds_write_b128 v240, v[48:51] offset:0\n"
  "v_cvt_f32_i32_e32 v28, v28\n"
  "v_cvt_f32_i32_e32 v29, v29\n"
  "v_cvt_f32_i32_e32 v30, v30\n"
  "v_cvt_f32_i32_e32 v31, v31\n"
  "v_cvt_f32_i32_e32 v36, v36\n"
  "v_cvt_f32_i32_e32 v37, v37\n"
  "v_cvt_f32_i32_e32 v38, v38\n"
  "v_cvt_f32_i32_e32 v39, v39\n"
  "v_pk_fma_f32 v[28:29], v[28:29], v[6:7], v[6:7] op_sel:[0,0,1] op_sel_hi:[1,0,1]\n"
  "v_pk_fma_f32 v[30:31], v[30:31], v[6:7], v[6:7] op_sel:[0,0,1] op_sel_hi:[1,0,1]\n"
  "v_pk_fma_f32 v[36:37], v[36:37], v[6:7], v[6:7] op_sel:[0,0,1] op_sel_hi:[1,0,1]\n"
  "v_pk_fma_f32 v[38:39], v[38:39], v[6:7], v[6:7] op_sel:[0,0,1] op_sel_hi:[1,0,1]\n"
  "v_pk_mul_f32 v[44:45], v[28:29], v[156:157]\n"
  "v_pk_mul_f32 v[40:41], v[36:37], v[148:149]\n"
  "v_pk_mul_f32 v[46:47], v[30:31], v[158:159]\n"
  "v_pk_mul_f32 v[42:43], v[38:39], v[150:151]\n"
  "v_pk_fma_f32 v[40:41], v[28:29], v[132:133], v[40:41] neg_lo:[0,0,1] neg_hi:[0,0,1]\n"
  "v_pk_fma_f32 v[44:45], v[36:37], v[140:141], v[44:45]\n"
  "v_pk_fma_f32 v[42:43], v[30:31], v[134:135], v[42:43] neg_lo:[0,0,1] neg_hi:[0,0,1]\n"
  "v_pk_fma_f32 v[46:47], v[38:39], v[142:143], v[46:47]\n"
  "v_cvt_pk_bf16_f32 v52, v40, v41\n"
  "v_cvt_pk_bf16_f32 v53, v42, v43\n"
  "v_cvt_pk_bf16_f32 v54, v44, v45\n"
  "v_cvt_pk_bf16_f32 v55, v46, v47\n"
  "ds_write_b128 v240, v[52:55] offset:1024\n"
  "ds_read_b128 v[8:11], v241\n"
  "ds_read_b128 v[12:15], v241 offset:1024\n"
  "v_add_u32_e32 v2, s71, v242\n"
  "s_cmp_lg_u32 s58, s44\n"
  "s_cbranch_scc1 L_psK_%=\n"
  "L_pbK_%=:\n"
  "s_branch L_go_%=\n"
  "L_psK_%=:\n"
  "s_waitcnt vmcnt(0)\n"
  "v_cmp_ne_u64_e32 vcc, s[50:51], v[222:223]\n"
  "s_and_b64 vcc, vcc, s[78:79]\n"
  "s_cbranch_scc0 L_pkK_%=\n"
  "s_mov_b32 s77, 0x10000\n"
  "L_spK_%=:\n"
  "buffer_load_dwordx2 v[222:223], v235, s[52:55], s57 offen sc1\n"
  "s_waitcnt vmcnt(0)\n"
  "v_cmp_ne_u64_e32 vcc, s[50:51], v[222:223]\n"
  "s_and_b64 vcc, vcc, s[78:79]\n"
  "s_cbranch_scc0 L_pkK_%=\n"
  "s_sub_u32 s77, s77, 1\n"
  "s_cmp_lg_u32 s77, 0\n"
  "s_cbranch_scc1 L_spK_%=\n"
  "s_branch L_pkK_%=\n"
  "L_pkK_%=:\n"
  "buffer_load_dwordx4 a[192:195], v224, s[40:43], s44 offen offset:0 sc1\n"
  "buffer_load_dwordx4 a[196:199], v224, s[40:43], s44 offen offset:1024 sc1\n"
  "buffer_load_dwordx4 a[200:203], v224, s[40:43], s44 offen offset:2048 sc1\n"
  "buffer_load_dwordx4 a[204:207], v224, s[40:43], s44 offen offset:3072 sc1\n"
  "buffer_load_dwordx4 a[208:211], v225, s[40:43], s44 offen offset:0 sc1\n"
  "buffer_load_dwordx4 a[212:215], v225, s[40:43], s44 offen offset:1024 sc1\n"
  "buffer_load_dwordx4 a[216:219], v225, s[40:43], s44 offen offset:2048 sc1\n"
  "buffer_load_dwordx4 a[220:223], v225, s[40:43], s44 offen offset:3072 sc1\n"
  "buffer_load_dwordx4 a[96:99], v226, s[40:43], s44 offen offset:0 sc1\n"
  "buffer_load_dwordx4 a[100:103], v226, s[40:43], s44 offen offset:1024 sc1\n"
  "buffer_load_dwordx4 a[104:107], v226, s[40:43], s44 offen offset:2048 sc1\n"
  "buffer_load_dwordx4 a[108:111], v226, s[40:43], s44 offen offset:3072 sc1\n"
  "buffer_load_dwordx4 a[112:115], v227, s[40:43], s44 offen offset:0 sc1\n"
  "buffer_load_dwordx4 a[116:119], v227, s[40:43], s44 offen offset:1024 sc1\n"
  "buffer_load_dwordx4 a[120:123], v227, s[40:43], s44 offen offset:2048 sc1\n"
  "buffer_load_dwordx4 a[124:127], v227, s[40:43], s44 offen offset:3072 sc1\n"
  "buffer_load_dwordx2 v[222:223], v235, s[52:55], s57 offen sc1\n"
  "s_branch L_pbK_%=\n"
  "L_psV_%=:\n"
  "s_waitcnt vmcnt(0)\n"
  "v_cmp_ne_u64_e32 vcc, s[50:51], v[222:223]\n"
  "s_and_b64 vcc, vcc, s[78:79]\n"
  "s_cbranch_scc0 L_pkV_%=\n"
  "s_mov_b32 s77, 0x10000\n"
  "L_spV_%=:\n"
  "buffer_load_dwordx2 v[222:223], v235, s[52:55], s57 offen sc1\n"
  "s_waitcnt vmcnt(0)\n"
  "v_cmp_ne_u64_e32 vcc, s[50:51], v[222:223]\n"
  "s_and_b64 vcc, vcc, s[78:79]\n"
  "s_cbranch_scc0 L_pkV_%=\n"
  "s_sub_u32 s77, s77, 1\n"
  "s_cmp_lg_u32 s77, 0\n"
  "s_cbranch_scc1 L_spV_%=\n"
  "s_branch L_pkV_%=\n"
  "L_pkV_%=:\n"
  "buffer_load_dwordx4 a[192:195], v224, s[40:43], s44 offen offset:0 sc1\n"
  "buffer_load_dwordx4 a[196:199], v224, s[40:43], s44 offen offset:1024 sc1\n"
  "buffer_load_dwordx4 a[200:203], v224, s[40:43], s44 offen offset:2048 sc1\n"
  "buffer_load_dwordx4 a[204:207], v224, s[40:43], s44 offen offset:3072 sc1\n"
  "buffer_load_dwordx4 a[208:211], v225, s[40:43], s44 offen offset:0 sc1\n"
  "buffer_load_dwordx4 a[212:215], v225, s[40:43], s44 offen offset:1024 sc1\n"
  "buffer_load_dwordx4 a[216:219], v225, s[40:43], s44 offen offset:2048 sc1\n"
  "buffer_load_dwordx4 a[220:223], v225, s[40:43], s44 offen offset:3072 sc1\n"
  "buffer_load_dwordx4 a[96:99], v226, s[40:43], s44 offen offset:0 sc1\n"
  "buffer_load_dwordx4 a[100:103], v226, s[40:43], s44 offen offset:1024 sc1\n"
  "buffer_load_dwordx4 a[104:107], v226, s[40:43], s44 offen offset:2048 sc1\n"
  "buffer_load_dwordx4 a[108:111], v226, s[40:43], s44 offen offset:3072 sc1\n"
  "buffer_load_dwordx4 a[112:115], v227, s[40:43], s44 offen offset:0 sc1\n"
  "buffer_load_dwordx4 a[116:119], v227, s[40:43], s44 offen offset:1024 sc1\n"
  "buffer_load_dwordx4 a[120:123], v227, s[40:43], s44 offen offset:2048 sc1\n"
  "buffer_load_dwordx4 a[124:127], v227, s[40:43], s44 offen offset:3072 sc1\n"
  "buffer_load_dwordx2 v[222:223], v235, s[52:55], s57 offen sc1\n"
  "s_branch L_pbV_%=\n"
  "L_redo_%=:\n"
  "s_cmp_eq_u32 s47, 0x7f800000\n"
  "s_cbranch_scc1 L_fin_%=\n"
  "s_mov_b32 s47, 0x7f800000\n"
  "s_mov_b32 s24, 0\n"
  "v_bfrev_b32_e32 v2, 1\n"
  "v_bfrev_b32_e32 v3, 1\n"
  "s_waitcnt vmcnt(0) lgkmcnt(0)\n"
  "s_nop 7\n"
  "L_mx_%=:\n"
  "buffer_load_dwordx4 a[192:195], v224, s[40:43], s24 offen offset:0 sc1\n"
  "buffer_load_dwordx4 a[196:199], v224, s[40:43], s24 offen offset:1024 sc1\n"
  "buffer_load_dwordx4 a[200:203], v224, s[40:43], s24 offen offset:2048 sc1\n"
  "buffer_load_dwordx4 a[204:207], v224, s[40:43], s24 offen offset:3072 sc1\n"
  "buffer_load_dwordx4 a[208:211], v225, s[40:43], s24 offen offset:0 sc1\n"
  "buffer_load_dwordx4 a[212:215], v225, s[40:43], s24 offen offset:1024 sc1\n"
  "buffer_load_dwordx4 a[216:219], v225, s[40:43], s24 offen offset:2048 sc1\n"
  "buffer_load_dwordx4 a[220:223], v225, s[40:43], s24 offen offset:3072 sc1\n"
  "s_waitcnt vmcnt(0)\n"
  "v_mfma_f32_16x16x32_bf16 v[128:131], a[192:195], a[128:131], 0\n"
  "v_mfma_f32_16x16x32_bf16 v[136:139], a[192:195], a[144:147], 0\n"
  "v_mfma_f32_16x16x32_bf16 v[144:147], a[192:195], a[160:163], 0\n"
  "v_mfma_f32_16x16x32_bf16 v[152:155], a[192:195], a[176:179], 0\n"
  "v_mfma_f32_16x16x32_bf16 v[128:131], a[196:199], a[132:135], v[128:131]\n"
  "v_mfma_f32_16x16x32_bf16 v[136:139], a[196:199], a[148:151], v[136:139]\n"
  "v_mfma_f32_16x16x32_bf16 v[144:147], a[196:199], a[164:167], v[144:147]\n"
  "v_mfma_f32_16x16x32_bf16 v[152:155], a[196:199], a[180:183], v[152:155]\n"
  "v_mfma_f32_16x16x32_bf16 v[128:131], a[200:203], a[136:139], v[128:131]\n"
  "v_mfma_f32_16x16x32_bf16 v[136:139], a[200:203], a[152:155], v[136:139]\n"
  "v_mfma_f32_16x16x32_bf16 v[144:147], a[200:203], a[168:171], v[144:147]\n"
  "v_mfma_f32_16x16x32_bf16 v[152:155], a[200:203], a[184:187], v[152:155]\n"
  "v_mfma_f32_16x16x32_bf16 v[128:131], a[204:207], a[140:143], v[128:131]\n"
  "v_mfma_f32_16x16x32_bf16 v[136:139], a[204:207], a[156:159], v[136:139]\n"
  "v_mfma_f32_16x16x32_bf16 v[144:147], a[204:207], a[172:175], v[144:147]\n"
  "v_mfma_f32_16x16x32_bf16 v[152:155], a[204:207], a[188:191], v[152:155]\n"
  "v_mfma_f32_16x16x32_bf16 v[132:135], a[208:211], a[128:131], 0\n"
  "v_mfma_f32_16x16x32_bf16 v[140:143], a[208:211], a[144:147], 0\n"
  "v_mfma_f32_16x16x32_bf16 v[148:151], a[208:211], a[160:163], 0\n"
  "v_mfma_f32_16x16x32_bf16 v[156:159], a[208:211], a[176:179], 0\n"
  "v_mfma_f32_16x16x32_bf16 v[132:135], a[212:215], a[132:135], v[132:135]\n"
  "v_mfma_f32_16x16x32_bf16 v[140:143], a[212:215], a[148:151], v[140:143]\n"
  "v_mfma_f32_16x16x32_bf16 v[148:151], a[212:215], a[164:167], v[148:151]\n"
  "v_mfma_f32_16x16x32_bf16 v[156:159], a[212:215], a[180:183], v[156:159]\n"
  "v_mfma_f32_16x16x32_bf16 v[132:135], a[216:219], a[136:139], v[132:135]\n"
  "v_mfma_f32_16x16x32_bf16 v[140:143], a[216:219], a[152:155], v[140:143]\n"
  "v_mfma_f32_16x16x32_bf16 v[148:151], a[216:219], a[168:171], v[148:151]\n"
  "v_mfma_f32_16x16x32_bf16 v[156:159], a[216:219], a[184:187], v[156:159]\n"
  "v_mfma_f32_16x16x32_bf16 v[132:135], a[220:223], a[140:143], v[132:135]\n"
  "v_mfma_f32_16x16x32_bf16 v[140:143], a[220:223], a[156:159], v[140:143]\n"
  "v_mfma_f32_16x16x32_bf16 v[148:151], a[220:223], a[172:175], v[148:151]\n"
  "v_mfma_f32_16x16x32_bf16 v[156:159], a[220:223], a[188:191], v[156:159]\n"
  "s_nop 7\n"
  "s_nop 1\n"
  "v_max3_f32 v212, v212, v128, v129\n"
  "v_max3_f32 v212, v212, v130, v131\n"
  "v_max3_f32 v212, v212, v132, v133\n"
  "v_max3_f32 v212, v212, v134, v135\n"
  "v_max3_f32 v213, v213, v136, v137\n"
  "v_max3_f32 v213, v213, v138, v139\n"
  "v_max3_f32 v213, v213, v140, v141\n"
  "v_max3_f32 v213, v213, v142, v143\n"
  "v_max3_f32 v214, v214, v144, v145\n"
  "v_max3_f32 v214, v214, v146, v147\n"
  "v_max3_f32 v214, v214, v148, v149\n"
  "v_max3_f32 v214, v214, v150, v151\n"
  "v_max3_f32 v215, v215, v152, v153\n"
  "v_max3_f32 v215, v215, v154, v155\n"
  "v_max3_f32 v215, v215, v156, v157\n"
  "v_max3_f32 v215, v215, v158, v159\n"
  "s_add_u32 s24, s24, 0x4000\n"
  "s_cmp_lt_u32 s24, 0x40000\n"
  "s_cbranch_scc1 L_mx_%=\n"
  "s_mov_b32 s46, 0x100\n"
  "s_mov_b32 s48, 0x4000\n"
  "s_mov_b32 s44, s56\n"
  "s_xor_b32 s45, s56, 0x4000\n"
  "s_mov_b32 s57, 0\n"
  "v_mov_b32_e32 v192, 0\n"
  "v_mov_b32_e32 v193, 0\n"
  "v_mov_b32_e32 v194, 0\n"
  "v_mov_b32_e32 v195, 0\n"
  "v_mov_b32_e32 v200, 0\n"
  "v_mov_b32_e32 v201, 0\n"
  "v_mov_b32_e32 v202, 0\n"
  "v_mov_b32_e32 v203, 0\n"
  "buffer_load_dwordx4 a[192:195], v224, s[40:43], s44 offen offset:0 sc1\n"
  "buffer_load_dwordx4 a[196:199], v224, s[40:43], s44 offen offset:1024 sc1\n"
  "buffer_load_dwordx4 a[200:203], v224, s[40:43], s44 offen offset:2048 sc1\n"
  "buffer_load_dwordx4 a[204:207], v224, s[40:43], s44 offen offset:3072 sc1\n"
  "buffer_load_dwordx4 a[208:211], v225, s[40:43], s44 offen offset:0 sc1\n"
  "buffer_load_dwordx4 a[212:215], v225, s[40:43], s44 offen offset:1024 sc1\n"
  "buffer_load_dwordx4 a[216:219], v225, s[40:43], s44 offen offset:2048 sc1\n"
  "buffer_load_dwordx4 a[220:223], v225, s[40:43], s44 offen offset:3072 sc1\n"
  "buffer_load_dwordx4 a[96:99], v226, s[40:43], s44 offen offset:0 sc1\n"
  "buffer_load_dwordx4 a[100:103], v226, s[40:43], s44 offen offset:1024 sc1\n"
  "buffer_load_dwordx4 a[104:107], v226, s[40:43], s44 offen offset:2048 sc1\n"
  "buffer_load_dwordx4 a[108:111], v226, s[40:43], s44 offen offset:3072 sc1\n"
  "buffer_load_dwordx4 a[112:115], v227, s[40:43], s44 offen offset:0 sc1\n"
  "buffer_load_dwordx4 a[116:119], v227, s[40:43], s44 offen offset:1024 sc1\n"
  "buffer_load_dwordx4 a[120:123], v227, s[40:43], s44 offen offset:2048 sc1\n"
  "buffer_load_dwordx4 a[124:127], v227, s[40:43], s44 offen offset:3072 sc1\n"
  "buffer_load_dwordx2 v[222:223], v235, s[52:55], s57 offen sc1\n"
  "s_waitcnt vmcnt(0)\n"
  "s_branch L_main_%=\n"
  "L_spin_%=:\n"
  "s_mov_b32 s77, 0x10000\n"
  "L_spL_%=:\n"
  "s_sleep 1\n"
  "buffer_load_dwordx2 v[222:223], v235, s[52:55], s57 offen sc1\n"
  "s_waitcnt vmcnt(0)\n"
  "v_cmp_ne_u64_e32 vcc, s[50:51], v[222:223]\n"
  "s_cbranch_vccz L_spinback_%=\n"
  "s_sub_u32 s77, s77, 1\n"
  "s_cmp_lg_u32 s77, 0\n"
  "s_cbranch_scc1 L_spL_%=\n"
  "s_branch L_spinback_%=\n"
  "L_spin0_%=:\n"
  "s_mov_b32 s77, 0x10000\n"
  "L_sp0_%=:\n"
  "s_sleep 1\n"
  "buffer_load_dwordx2 v[222:223], v235, s[52:55], s57 offen sc1\n"
  "s_waitcnt vmcnt(0)\n"
  "v_cmp_ne_u64_e32 vcc, s[50:51], v[222:223]\n"
  "s_cbranch_vccz L_spin0back_%=\n"
  "s_sub_u32 s77, s77, 1\n"
  "s_cmp_lg_u32 s77, 0\n"
  "s_cbranch_scc1 L_sp0_%=\n"
  "s_branch L_spin0back_%=\n"
  "L_spin2_%=:\n"
  "s_mov_b32 s77, 0x10000\n"
  "L_sp2_%=:\n"
  "s_sleep 1\n"
  "buffer_load_dwordx2 v[222:223], v235, s[52:55], s57 offen sc1\n"
  "s_waitcnt vmcnt(0)\n"
  "v_cmp_ne_u64_e32 vcc, s[50:51], v[222:223]\n"
  "s_cbranch_vccz L_spin2back_%=\n"
  "s_sub_u32 s77, s77, 1\n"
  "s_cmp_lg_u32 s77, 0\n"
  "s_cbranch_scc1 L_sp2_%=\n"
  "s_branch L_spin2back_%=\n"
  "L_vpath_%=:\n"
  "s_mov_b64 s[24:25], s[80:81]\n"
  "s_mov_b64 s[16:17], s[94:95]\n"
  "s_mov_b64 s[20:21], s[96:97]\n"
  "s_mov_b32 s26, 4194304\n"
  "s_mov_b32 s27, 0x20000\n"
  "s_mov_b32 s42, 262144\n"
  "s_mov_b32 s43, 0x20000\n"
  "s_mov_b32 s54, 1024\n"
  "s_mov_b32 s55, 0x20000\n"
  "s_add_u32 s40, s40, s62\n"
  "s_addc_u32 s41, s63, 0\n"
  "s_add_u32 s52, s52, s62\n"
  "s_addc_u32 s53, s63, 0\n"
  "v_add_u32_e32 v225, 0x1000, v224\n"
  "v_add_u32_e32 v227, 0x1000, v226\n"
  "s_mov_b32 s47, 0x53800000\n"
  "s_mov_b32 s46, 0x100\n"
  "s_mov_b32 s48, 0x4000\n"
  "s_mov_b32 s44, s56\n"
  "s_xor_b32 s45, s56, 0x4000\n"
  "s_waitcnt vmcnt(10)\n"
  "v_readfirstlane_b32 s78, v0\n"
  "v_readfirstlane_b32 s79, v1\n"
  "s_mul_i32 s57, s79, 0x4C957F2D\n"
  "s_mul_i32 s58, s78, 0x5851F42D\n"
  "s_mul_hi_u32 s51, s78, 0x4C957F2D\n"
  "s_mul_i32 s50, s78, 0x4C957F2D\n"
  "s_add_u32 s51, s51, s57\n"
  "s_add_u32 s51, s51, s58\n"
  "s_add_u32 s50, s50, 0xF767814F\n"
  "s_addc_u32 s51, s51, 0x14057B7E\n"
  "v_mov_b32_e32 v248, s50\n"
  "v_mov_b32_e32 v249, s51\n"
  "s_mov_b32 s57, 0\n"
  "s_lshr_b32 s78, s56, 11\n"
  "s_and_b32 s78, s78, 16\n"
  "s_lshl_b32 s78, 0xffff, s78\n"
  "s_mov_b32 s79, 0\n"
  "s_barrier\n"
  "s_waitcnt vmcnt(8)\n"
  "v_lshlrev_b32_e32 v128, 16, v8\n"
  "v_and_b32_e32 v129, 0xffff0000, v8\n"
  "v_lshlrev_b32_e32 v136, 16, v12\n"
  "v_and_b32_e32 v137, 0xffff0000, v12\n"
  "v_lshlrev_b32_e32 v130, 16, v9\n"
  "v_and_b32_e32 v131, 0xffff0000, v9\n"
  "v_lshlrev_b32_e32 v138, 16, v13\n"
  "v_and_b32_e32 v139, 0xffff0000, v13\n"
  "v_lshlrev_b32_e32 v132, 16, v10\n"
  "v_and_b32_e32 v133, 0xffff0000, v10\n"
  "v_lshlrev_b32_e32 v140, 16, v14\n"
  "v_and_b32_e32 v141, 0xffff0000, v14\n"
  "v_lshlrev_b32_e32 v134, 16, v11\n"
  "v_and_b32_e32 v135, 0xffff0000, v11\n"
  "v_lshlrev_b32_e32 v142, 16, v15\n"
  "v_and_b32_e32 v143, 0xffff0000, v15\n"
  "v_pk_mul_f32 v[136:137], v[136:137], v[128:129] neg_lo:[1,0] neg_hi:[1,0]\n"
  "v_pk_mul_f32 v[138:139], v[138:139], v[130:131] neg_lo:[1,0] neg_hi:[1,0]\n"
  "v_pk_mul_f32 v[140:141], v[140:141], v[132:133] neg_lo:[1,0] neg_hi:[1,0]\n"
  "v_pk_mul_f32 v[142:143], v[142:143], v[134:135] neg_lo:[1,0] neg_hi:[1,0]\n"
  "s_waitcnt vmcnt(6)\n"
  "v_cvt_f32_i32_e32 v56, v64\n"
  "v_cvt_f32_i32_e32 v57, v68\n"
  "v_cvt_f32_i32_e32 v58, v65\n"
  "v_cvt_f32_i32_e32 v59, v69\n"
  "v_cvt_f32_i32_e32 v60, v66\n"
  "v_cvt_f32_i32_e32 v61, v70\n"
  "v_cvt_f32_i32_e32 v62, v67\n"
  "v_cvt_f32_i32_e32 v63, v71\n"
  "v_pk_fma_f32 v[56:57], v[56:57], v[128:129], v[136:137]\n"
  "v_pk_fma_f32 v[58:59], v[58:59], v[128:129], v[136:137]\n"
  "v_pk_fma_f32 v[60:61], v[60:61], v[128:129], v[136:137]\n"
  "v_pk_fma_f32 v[62:63], v[62:63], v[128:129], v[136:137]\n"
  "v_cvt_pk_bf16_f32 v144, v56, v57\n"
  "v_cvt_pk_bf16_f32 v148, v58, v59\n"
  "v_cvt_pk_bf16_f32 v152, v60, v61\n"
  "v_cvt_pk_bf16_f32 v156, v62, v63\n"
  "s_waitcnt vmcnt(4)\n"
  "v_cvt_f32_i32_e32 v56, v72\n"
  "v_cvt_f32_i32_e32 v57, v76\n"
  "v_cvt_f32_i32_e32 v58, v73\n"
  "v_cvt_f32_i32_e32 v59, v77\n"
  "v_cvt_f32_i32_e32 v60, v74\n"
  "v_cvt_f32_i32_e32 v61, v78\n"
  "v_cvt_f32_i32_e32 v62, v75\n"
  "v_cvt_f32_i32_e32 v63, v79\n"
  "v_pk_fma_f32 v[56:57], v[56:57], v[130:131], v[138:139]\n"
  "v_pk_fma_f32 v[58:59], v[58:59], v[130:131], v[138:139]\n"
  "v_pk_fma_f32 v[60:61], v[60:61], v[130:131], v[138:139]\n"
  "v_pk_fma_f32 v[62:63], v[62:63], v[130:131], v[138:139]\n"
  "v_cvt_pk_bf16_f32 v145, v56, v57\n"
  "v_cvt_pk_bf16_f32 v149, v58, v59\n"
  "v_cvt_pk_bf16_f32 v153, v60, v61\n"
  "v_cvt_pk_bf16_f32 v157, v62, v63\n"
  "s_waitcnt vmcnt(2)\n"
  "v_cvt_f32_i32_e32 v56, v80\n"
  "v_cvt_f32_i32_e32 v57, v84\n"
  "v_cvt_f32_i32_e32 v58, v81\n"
  "v_cvt_f32_i32_e32 v59, v85\n"
  "v_cvt_f32_i32_e32 v60, v82\n"
  "v_cvt_f32_i32_e32 v61, v86\n"
  "v_cvt_f32_i32_e32 v62, v83\n"
  "v_cvt_f32_i32_e32 v63, v87\n"
  "v_pk_fma_f32 v[56:57], v[56:57], v[132:133], v[140:141]\n"
  "v_pk_fma_f32 v[58:59], v[58:59], v[132:133], v[140:141]\n"
  "v_pk_fma_f32 v[60:61], v[60:61], v[132:133], v[140:141]\n"
  "v_pk_fma_f32 v[62:63], v[62:63], v[132:133], v[140:141]\n"
  "v_cvt_pk_bf16_f32 v146, v56, v57\n"
  "v_cvt_pk_bf16_f32 v150, v58, v59\n"
  "v_cvt_pk_bf16_f32 v154, v60, v61\n"
  "v_cvt_pk_bf16_f32 v158, v62, v63\n"
  "s_waitcnt vmcnt(0)\n"
  "v_cvt_f32_i32_e32 v56, v88\n"
  "v_cvt_f32_i32_e32 v57, v92\n"
  "v_cvt_f32_i32_e32 v58, v89\n"
  "v_cvt_f32_i32_e32 v59, v93\n"
  "v_cvt_f32_i32_e32 v60, v90\n"
  "v_cvt_f32_i32_e32 v61, v94\n"
  "v_cvt_f32_i32_e32 v62, v91\n"
  "v_cvt_f32_i32_e32 v63, v95\n"
  "v_pk_fma_f32 v[56:57], v[56:57], v[134:135], v[142:143]\n"
  "v_pk_fma_f32 v[58:59], v[58:59], v[134:135], v[142:143]\n"
  "v_pk_fma_f32 v[60:61], v[60:61], v[134:135], v[142:143]\n"
  "v_pk_fma_f32 v[62:63], v[62:63], v[134:135], v[142:143]\n"
  "v_cvt_pk_bf16_f32 v147, v56, v57\n"
  "v_cvt_pk_bf16_f32 v151, v58, v59\n"
  "v_cvt_pk_bf16_f32 v155, v60, v61\n"
  "v_cvt_pk_bf16_f32 v159, v62, v63\n"
  "buffer_store_dwordx4 v[144:147], v242, s[36:39], s68 offen offset:0 sc1\n"
  "buffer_store_dwordx4 v[148:151], v242, s[36:39], s68 offen offset:1024 sc1\n"
  "buffer_store_dwordx4 v[152:155], v242, s[36:39], s68 offen offset:2048 sc1\n"
  "buffer_store_dwordx4 v[156:159], v242, s[36:39], s68 offen offset:3072 sc1\n"
  "s_lshr_b32 s74, s65, 8\n"
  "buffer_load_dwordx4 v[160:163], v239, s[24:27], s76 offen offset:0 nt\n"
  "buffer_load_dwordx4 v[164:167], v239, s[24:27], s76 offen offset:128 nt\n"
  "buffer_load_dwordx4 v[168:171], v239, s[16:19], 0 offen offset:0\n"
  "buffer_load_dwordx4 v[172:175], v239, s[16:19], 0 offen offset:128\n"
  "buffer_load_dwordx4 v[176:179], v239, s[20:23], 0 offen offset:0\n"
  "buffer_load_dwordx4 v[180:183], v239, s[20:23], 0 offen offset:128\n"
  "s_waitcnt vmcnt(6)\n"
  "buffer_store_dwordx2 v[248:249], v244, s[36:39], s72 offen sc1\n"
  "buffer_load_dwordx4 v[184:187], v239, s[24:27], s76 offen offset:2048 nt\n"
  "buffer_load_dwordx4 v[188:191], v239, s[24:27], s76 offen offset:2176 nt\n"
  "buffer_load_dwordx4 v[192:195], v239, s[16:19], 0 offen offset:2048\n"
  "buffer_load_dwordx4 v[196:199], v239, s[16:19], 0 offen offset:2176\n"
  "buffer_load_dwordx4 v[200:203], v239, s[20:23], 0 offen offset:2048\n"
  "buffer_load_dwordx4 v[204:207], v239, s[20:23], 0 offen offset:2176\n"
  "s_waitcnt vmcnt(7)\n"
  "v_lshlrev_b32_e32 v208, 16, v160\n"
  "v_and_b32_e32 v209, 0xffff0000, v160\n"
  "v_lshlrev_b32_e32 v210, 16, v164\n"
  "v_and_b32_e32 v211, 0xffff0000, v164\n"
  "v_lshlrev_b32_e32 v216, 16, v176\n"
  "v_and_b32_e32 v217, 0xffff0000, v176\n"
  "v_lshlrev_b32_e32 v218, 16, v180\n"
  "v_and_b32_e32 v219, 0xffff0000, v180\n"
  "v_lshlrev_b32_e32 v212, 16, v168\n"
  "v_and_b32_e32 v213, 0xffff0000, v168\n"
  "v_lshlrev_b32_e32 v214, 16, v172\n"
  "v_and_b32_e32 v215, 0xffff0000, v172\n"
  "v_pk_mul_f32 v[216:217], v[210:211], v[216:217]\n"
  "v_pk_mul_f32 v[218:219], v[208:209], v[218:219]\n"
  "v_pk_fma_f32 v[216:217], v[208:209], v[212:213], v[216:217] neg_lo:[0,0,1] neg_hi:[0,0,1]\n"
  "v_pk_fma_f32 v[218:219], v[210:211], v[214:215], v[218:219]\n"
  "v_cvt_pk_bf16_f32 v112, v216, v217\n"
  "v_cvt_pk_bf16_f32 v114, v218, v219\n"
  "v_lshlrev_b32_e32 v208, 16, v161\n"
  "v_and_b32_e32 v209, 0xffff0000, v161\n"
  "v_lshlrev_b32_e32 v210, 16, v165\n"
  "v_and_b32_e32 v211, 0xffff0000, v165\n"
  "v_lshlrev_b32_e32 v216, 16, v177\n"
  "v_and_b32_e32 v217, 0xffff0000, v177\n"
  "v_lshlrev_b32_e32 v218, 16, v181\n"
  "v_and_b32_e32 v219, 0xffff0000, v181\n"
  "v_lshlrev_b32_e32 v212, 16, v169\n"
  "v_and_b32_e32 v213, 0xffff0000, v169\n"
  "v_lshlrev_b32_e32 v214, 16, v173\n"
  "v_and_b32_e32 v215, 0xffff0000, v173\n"
  "v_pk_mul_f32 v[216:217], v[210:211], v[216:217]\n"
  "v_pk_mul_f32 v[218:219], v[208:209], v[218:219]\n"
  "v_pk_fma_f32 v[216:217], v[208:209], v[212:213], v[216:217] neg_lo:[0,0,1] neg_hi:[0,0,1]\n"
  "v_pk_fma_f32 v[218:219], v[210:211], v[214:215], v[218:219]\n"
  "v_cvt_pk_bf16_f32 v113, v216, v217\n"
  "v_cvt_pk_bf16_f32 v115, v218, v219\n"
  "v_lshlrev_b32_e32 v208, 16, v162\n"
  "v_and_b32_e32 v209, 0xffff0000, v162\n"
  "v_lshlrev_b32_e32 v210, 16, v166\n"
  "v_and_b32_e32 v211, 0xffff0000, v166\n"
  "v_lshlrev_b32_e32 v216, 16, v178\n"
  "v_and_b32_e32 v217, 0xffff0000, v178\n"
  "v_lshlrev_b32_e32 v218, 16, v182\n"
  "v_and_b32_e32 v219, 0xffff0000, v182\n"
  "v_lshlrev_b32_e32 v212, 16, v170\n"
  "v_and_b32_e32 v213, 0xffff0000, v170\n"
  "v_lshlrev_b32_e32 v214, 16, v174\n"
  "v_and_b32_e32 v215, 0xffff0000, v174\n"
  "v_pk_mul_f32 v[216:217], v[210:211], v[216:217]\n"
  "v_pk_mul_f32 v[218:219], v[208:209], v[218:219]\n"
  "v_pk_fma_f32 v[216:217], v[208:209], v[212:213], v[216:217] neg_lo:[0,0,1] neg_hi:[0,0,1]\n"
  "v_pk_fma_f32 v[218:219], v[210:211], v[214:215], v[218:219]\n"
  "v_cvt_pk_bf16_f32 v116, v216, v217\n"
  "v_cvt_pk_bf16_f32 v118, v218, v219\n"
  "v_lshlrev_b32_e32 v208, 16, v163\n"
  "v_and_b32_e32 v209, 0xffff0000, v163\n"
  "v_lshlrev_b32_e32 v210, 16, v167\n"
  "v_and_b32_e32 v211, 0xffff0000, v167\n"
  "v_lshlrev_b32_e32 v216, 16, v179\n"
  "v_and_b32_e32 v217, 0xffff0000, v179\n"
  "v_lshlrev_b32_e32 v218, 16, v183\n"
  "v_and_b32_e32 v219, 0xffff0000, v183\n"
  "v_lshlrev_b32_e32 v212, 16, v171\n"
  "v_and_b32_e32 v213, 0xffff0000, v171\n"
  "v_lshlrev_b32_e32 v214, 16, v175\n"
  "v_and_b32_e32 v215, 0xffff0000, v175\n"
  "v_pk_mul_f32 v[216:217], v[210:211], v[216:217]\n"
  "v_pk_mul_f32 v[218:219], v[208:209], v[218:219]\n"
  "v_pk_fma_f32 v[216:217], v[208:209], v[212:213], v[216:217] neg_lo:[0,0,1] neg_hi:[0,0,1]\n"
  "v_pk_fma_f32 v[218:219], v[210:211], v[214:215], v[218:219]\n"
  "v_cvt_pk_bf16_f32 v117, v216, v217\n"
  "v_cvt_pk_bf16_f32 v119, v218, v219\n"
  "ds_write_b128 v243, v[112:115] offset:0\n"
  "ds_write_b128 v243, v[116:119] offset:256\n"
  "buffer_load_dwordx4 v[16:19], v238, s[8:11], s74 offen\n"
  "buffer_load_dwordx4 v[20:23], v238, s[12:15], s74 offen\n"
  "buffer_load_dwordx4 v[24:27], v236, s[4:7], s65 offen offset:0 nt\n"
  "buffer_load_dwordx4 v[28:31], v236, s[4:7], s65 offen offset:512 nt\n"
  "buffer_load_dwordx4 v[32:35], v236, s[4:7], s65 offen offset:1024 nt\n"
  "buffer_load_dwordx4 v[36:39], v236, s[4:7], s65 offen offset:1536 nt\n"
  "buffer_load_dwordx4 v[40:43], v236, s[4:7], s65 offen offset:2048 nt\n"
  "buffer_load_dwordx4 v[44:47], v236, s[4:7], s65 offen offset:2560 nt\n"
  "buffer_load_dwordx4 v[48:51], v236, s[4:7], s65 offen offset:3072 nt\n"
  "buffer_load_dwordx4 v[52:55], v236, s[4:7], s65 offen offset:3584 nt\n"
  "s_waitcnt vmcnt(10)\n"
  "v_lshlrev_b32_e32 v208, 16, v184\n"
  "v_and_b32_e32 v209, 0xffff0000, v184\n"
  "v_lshlrev_b32_e32 v210, 16, v188\n"
  "v_and_b32_e32 v211, 0xffff0000, v188\n"
  "v_lshlrev_b32_e32 v216, 16, v200\n"
  "v_and_b32_e32 v217, 0xffff0000, v200\n"
  "v_lshlrev_b32_e32 v218, 16, v204\n"
  "v_and_b32_e32 v219, 0xffff0000, v204\n"
  "v_lshlrev_b32_e32 v212, 16, v192\n"
  "v_and_b32_e32 v213, 0xffff0000, v192\n"
  "v_lshlrev_b32_e32 v214, 16, v196\n"
  "v_and_b32_e32 v215, 0xffff0000, v196\n"
  "v_pk_mul_f32 v[216:217], v[210:211], v[216:217]\n"
  "v_pk_mul_f32 v[218:219], v[208:209], v[218:219]\n"
  "v_pk_fma_f32 v[216:217], v[208:209], v[212:213], v[216:217] neg_lo:[0,0,1] neg_hi:[0,0,1]\n"
  "v_pk_fma_f32 v[218:219], v[210:211], v[214:215], v[218:219]\n"
  "v_cvt_pk_bf16_f32 v120, v216, v217\n"
  "v_cvt_pk_bf16_f32 v122, v218, v219\n"
  "v_lshlrev_b32_e32 v208, 16, v185\n"
  "v_and_b32_e32 v209, 0xffff0000, v185\n"
  "v_lshlrev_b32_e32 v210, 16, v189\n"
  "v_and_b32_e32 v211, 0xffff0000, v189\n"
  "v_lshlrev_b32_e32 v216, 16, v201\n"
  "v_and_b32_e32 v217, 0xffff0000, v201\n"
  "v_lshlrev_b32_e32 v218, 16, v205\n"
  "v_and_b32_e32 v219, 0xffff0000, v205\n"
  "v_lshlrev_b32_e32 v212, 16, v193\n"
  "v_and_b32_e32 v213, 0xffff0000, v193\n"
  "v_lshlrev_b32_e32 v214, 16, v197\n"
  "v_and_b32_e32 v215, 0xffff0000, v197\n"
  "v_pk_mul_f32 v[216:217], v[210:211], v[216:217]\n"
  "v_pk_mul_f32 v[218:219], v[208:209], v[218:219]\n"
  "v_pk_fma_f32 v[216:217], v[208:209], v[212:213], v[216:217] neg_lo:[0,0,1] neg_hi:[0,0,1]\n"
  "v_pk_fma_f32 v[218:219], v[210:211], v[214:215], v[218:219]\n"
  "v_cvt_pk_bf16_f32 v121, v216, v217\n"
  "v_cvt_pk_bf16_f32 v123, v218, v219\n"
  "v_lshlrev_b32_e32 v208, 16, v186\n"
  "v_and_b32_e32 v209, 0xffff0000, v186\n"
  "v_lshlrev_b32_e32 v210, 16, v190\n"
  "v_and_b32_e32 v211, 0xffff0000, v190\n"
  "v_lshlrev_b32_e32 v216, 16, v202\n"
  "v_and_b32_e32 v217, 0xffff0000, v202\n"
  "v_lshlrev_b32_e32 v218, 16, v206\n"
  "v_and_b32_e32 v219, 0xffff0000, v206\n"
  "v_lshlrev_b32_e32 v212, 16, v194\n"
  "v_and_b32_e32 v213, 0xffff0000, v194\n"
  "v_lshlrev_b32_e32 v214, 16, v198\n"
  "v_and_b32_e32 v215, 0xffff0000, v198\n"
  "v_pk_mul_f32 v[216:217], v[210:211], v[216:217]\n"
  "v_pk_mul_f32 v[218:219], v[208:209], v[218:219]\n"
  "v_pk_fma_f32 v[216:217], v[208:209], v[212:213], v[216:217] neg_lo:[0,0,1] neg_hi:[0,0,1]\n"
  "v_pk_fma_f32 v[218:219], v[210:211], v[214:215], v[218:219]\n"
  "v_cvt_pk_bf16_f32 v124, v216, v217\n"
  "v_cvt_pk_bf16_f32 v126, v218, v219\n"
  "v_lshlrev_b32_e32 v208, 16, v187\n"
  "v_and_b32_e32 v209, 0xffff0000, v187\n"
  "v_lshlrev_b32_e32 v210, 16, v191\n"
  "v_and_b32_e32 v211, 0xffff0000, v191\n"
  "v_lshlrev_b32_e32 v216, 16, v203\n"
  "v_and_b32_e32 v217, 0xffff0000, v203\n"
  "v_lshlrev_b32_e32 v218, 16, v207\n"
  "v_and_b32_e32 v219, 0xffff0000, v207\n"
  "v_lshlrev_b32_e32 v212, 16, v195\n"
  "v_and_b32_e32 v213, 0xffff0000, v195\n"
  "v_lshlrev_b32_e32 v214, 16, v199\n"
  "v_and_b32_e32 v215, 0xffff0000, v199\n"
  "v_pk_mul_f32 v[216:217], v[210:211], v[216:217]\n"
  "v_pk_mul_f32 v[218:219], v[208:209], v[218:219]\n"
  "v_pk_fma_f32 v[216:217], v[208:209], v[212:213], v[216:217] neg_lo:[0,0,1] neg_hi:[0,0,1]\n"
  "v_pk_fma_f32 v[218:219], v[210:211], v[214:215], v[218:219]\n"
  "v_cvt_pk_bf16_f32 v125, v216, v217\n"
  "v_cvt_pk_bf16_f32 v127, v218, v219\n"
  "ds_write_b128 v243, v[120:123] offset:128\n"
  "ds_write_b128 v243, v[124:127] offset:384\n"
  "s_waitcnt vmcnt(8)\n"
  "v_lshlrev_b32_e32 v128, 16, v16\n"
  "v_and_b32_e32 v129, 0xffff0000, v16\n"
  "v_lshlrev_b32_e32 v136, 16, v20\n"
  "v_and_b32_e32 v137, 0xffff0000, v20\n"
  "v_lshlrev_b32_e32 v130, 16, v17\n"
  "v_and_b32_e32 v131, 0xffff0000, v17\n"
  "v_lshlrev_b32_e32 v138, 16, v21\n"
  "v_and_b32_e32 v139, 0xffff0000, v21\n"
  "v_lshlrev_b32_e32 v132, 16, v18\n"
  "v_and_b32_e32 v133, 0xffff0000, v18\n"
  "v_lshlrev_b32_e32 v140, 16, v22\n"
  "v_and_b32_e32 v141, 0xffff0000, v22\n"
  "v_lshlrev_b32_e32 v134, 16, v19\n"
  "v_and_b32_e32 v135, 0xffff0000, v19\n"
  "v_lshlrev_b32_e32 v142, 16, v23\n"
  "v_and_b32_e32 v143, 0xffff0000, v23\n"
  "v_pk_mul_f32 v[136:137], v[136:137], v[128:129] neg_lo:[1,0] neg_hi:[1,0]\n"
  "v_pk_mul_f32 v[138:139], v[138:139], v[130:131] neg_lo:[1,0] neg_hi:[1,0]\n"
  "v_pk_mul_f32 v[140:141], v[140:141], v[132:133] neg_lo:[1,0] neg_hi:[1,0]\n"
  "v_pk_mul_f32 v[142:143], v[142:143], v[134:135] neg_lo:[1,0] neg_hi:[1,0]\n"
  "s_waitcnt vmcnt(6)\n"
  "v_cvt_f32_i32_e32 v56, v24\n"
  "v_cvt_f32_i32_e32 v57, v28\n"
  "v_cvt_f32_i32_e32 v58, v25\n"
  "v_cvt_f32_i32_e32 v59, v29\n"
  "v_cvt_f32_i32_e32 v60, v26\n"
  "v_cvt_f32_i32_e32 v61, v30\n"
  "v_cvt_f32_i32_e32 v62, v27\n"
  "v_cvt_f32_i32_e32 v63, v31\n"
  "v_pk_fma_f32 v[56:57], v[56:57], v[128:129], v[136:137]\n"
  "v_pk_fma_f32 v[58:59], v[58:59], v[128:129], v[136:137]\n"
  "v_pk_fma_f32 v[60:61], v[60:61], v[128:129], v[136:137]\n"
  "v_pk_fma_f32 v[62:63], v[62:63], v[128:129], v[136:137]\n"
  "v_cvt_pk_bf16_f32 v8, v56, v57\n"
  "v_cvt_pk_bf16_f32 v16, v58, v59\n"
  "v_cvt_pk_bf16_f32 v12, v60, v61\n"
  "v_cvt_pk_bf16_f32 v20, v62, v63\n"
  "s_waitcnt vmcnt(4)\n"
  "v_cvt_f32_i32_e32 v56, v32\n"
  "v_cvt_f32_i32_e32 v57, v36\n"
  "v_cvt_f32_i32_e32 v58, v33\n"
  "v_cvt_f32_i32_e32 v59, v37\n"
  "v_cvt_f32_i32_e32 v60, v34\n"
  "v_cvt_f32_i32_e32 v61, v38\n"
  "v_cvt_f32_i32_e32 v62, v35\n"
  "v_cvt_f32_i32_e32 v63, v39\n"
  "v_pk_fma_f32 v[56:57], v[56:57], v[130:131], v[138:139]\n"
  "v_pk_fma_f32 v[58:59], v[58:59], v[130:131], v[138:139]\n"
  "v_pk_fma_f32 v[60:61], v[60:61], v[130:131], v[138:139]\n"
  "v_pk_fma_f32 v[62:63], v[62:63], v[130:131], v[138:139]\n"
  "v_cvt_pk_bf16_f32 v9, v56, v57\n"
  "v_cvt_pk_bf16_f32 v17, v58, v59\n"
  "v_cvt_pk_bf16_f32 v13, v60, v61\n"
  "v_cvt_pk_bf16_f32 v21, v62, v63\n"
  "buffer_load_dwordx2 v[222:223], v235, s[52:55], s57 offen sc1\n"
  "s_waitcnt lgkmcnt(0)\n"
  "s_barrier\n"
  "ds_read_b128 a[128:131], v228 offset:0\n"
  "ds_read_b128 a[132:135], v228 offset:1056\n"
  "ds_read_b128 a[136:139], v228 offset:2112\n"
  "ds_read_b128 a[140:143], v228 offset:3168\n"
  "ds_read_b128 a[144:147], v229 offset:0\n"
  "ds_read_b128 a[148:151], v229 offset:1056\n"
  "ds_read_b128 a[152:155], v229 offset:2112\n"
  "ds_read_b128 a[156:159], v229 offset:3168\n"
  "ds_read_b128 a[160:163], v230 offset:0\n"
  "ds_read_b128 a[164:167], v230 offset:1056\n"
  "ds_read_b128 a[168:171], v230 offset:2112\n"
  "ds_read_b128 a[172:175], v230 offset:3168\n"
  "ds_read_b128 a[176:179], v231 offset:0\n"
  "ds_read_b128 a[180:183], v231 offset:1056\n"
  "ds_read_b128 a[184:187], v231 offset:2112\n"
  "ds_read_b128 a[188:191], v231 offset:3168\n"
  "v_mov_b32_e32 v192, 0\n"
  "v_mov_b32_e32 v193, 0\n"
  "v_mov_b32_e32 v194, 0\n"
  "v_mov_b32_e32 v195, 0\n"
  "v_mov_b32_e32 v200, 0\n"
  "v_mov_b32_e32 v201, 0\n"
  "v_mov_b32_e32 v202, 0\n"
  "v_mov_b32_e32 v203, 0\n"
  "v_mov_b32_e32 v208, v246\n"
  "v_mov_b32_e32 v209, v246\n"
  "v_mov_b32_e32 v210, v246\n"
  "v_mov_b32_e32 v211, v246\n"
  "v_mov_b32_e32 v212, 0xff800000\n"
  "v_mov_b32_e32 v213, 0xff800000\n"
  "v_mov_b32_e32 v214, 0xff800000\n"
  "v_mov_b32_e32 v215, 0xff800000\n"
  "s_waitcnt vmcnt(0)\n"
  "v_cmp_ne_u64_e32 vcc, s[50:51], v[222:223]\n"
  "s_and_b64 vcc, vcc, s[78:79]\n"
  "s_cselect_b32 s58, 0x7ff00000, s44\n"
  "buffer_load_dwordx4 a[192:195], v224, s[40:43], s58 offen offset:0 sc1\n"
  "buffer_load_dwordx4 a[196:199], v224, s[40:43], s58 offen offset:1024 sc1\n"
  "buffer_load_dwordx4 a[200:203], v224, s[40:43], s58 offen offset:2048 sc1\n"
  "buffer_load_dwordx4 a[204:207], v224, s[40:43], s58 offen offset:3072 sc1\n"
  "v_cvt_f32_i32_e32 v56, v40\n"
  "v_cvt_f32_i32_e32 v57, v44\n"
  "v_cvt_f32_i32_e32 v58, v41\n"
  "v_cvt_f32_i32_e32 v59, v45\n"
  "v_cvt_f32_i32_e32 v60, v42\n"
  "buffer_load_dwordx4 a[208:211], v225, s[40:43], s58 offen offset:0 sc1\n"
  "v_cvt_f32_i32_e32 v61, v46\n"
  "v_cvt_f32_i32_e32 v62, v43\n"
  "v_cvt_f32_i32_e32 v63, v47\n"
  "v_pk_fma_f32 v[56:57], v[56:57], v[132:133], v[140:141]\n"
  "v_pk_fma_f32 v[58:59], v[58:59], v[132:133], v[140:141]\n"
  "buffer_load_dwordx4 a[212:215], v225, s[40:43], s58 offen offset:1024 sc1\n"
  "v_pk_fma_f32 v[60:61], v[60:61], v[132:133], v[140:141]\n"
  "v_pk_fma_f32 v[62:63], v[62:63], v[132:133], v[140:141]\n"
  "v_cvt_pk_bf16_f32 v10, v56, v57\n"
  "v_cvt_pk_bf16_f32 v18, v58, v59\n"
  "v_cvt_pk_bf16_f32 v14, v60, v61\n"
  "buffer_load_dwordx4 a[216:219], v225, s[40:43], s58 offen offset:2048 sc1\n"
  "v_cvt_pk_bf16_f32 v22, v62, v63\n"
  "v_cvt_f32_i32_e32 v56, v48\n"
  "v_cvt_f32_i32_e32 v57, v52\n"
  "v_cvt_f32_i32_e32 v58, v49\n"
  "v_cvt_f32_i32_e32 v59, v53\n"
  "buffer_load_dwordx4 a[220:223], v225, s[40:43], s58 offen offset:3072 sc1\n"
  "v_cvt_f32_i32_e32 v60, v50\n"
  "v_cvt_f32_i32_e32 v61, v54\n"
  "v_cvt_f32_i32_e32 v62, v51\n"
  "v_cvt_f32_i32_e32 v63, v55\n"
  "v_pk_fma_f32 v[56:57], v[56:57], v[134:135], v[142:143]\n"
  "buffer_load_dwordx4 a[96:99], v226, s[40:43], s58 offen offset:0 sc1\n"
  "v_pk_fma_f32 v[58:59], v[58:59], v[134:135], v[142:143]\n"
  "v_pk_fma_f32 v[60:61], v[60:61], v[134:135], v[142:143]\n"
  "v_pk_fma_f32 v[62:63], v[62:63], v[134:135], v[142:143]\n"
  "v_cvt_pk_bf16_f32 v11, v56, v57\n"
  "v_cvt_pk_bf16_f32 v19, v58, v59\n"
  "buffer_load_dwordx4 a[100:103], v226, s[40:43], s58 offen offset:1024 sc1\n"
  "v_cvt_pk_bf16_f32 v15, v60, v61\n"
  "v_cvt_pk_bf16_f32 v23, v62, v63\n"
  "v_add_u32_e32 v2, s69, v242\n"
  "v_add_u32_e32 v3, 0x400, v2\n"
  "buffer_load_dwordx4 a[104:107], v226, s[40:43], s58 offen offset:2048 sc1\n"
  "buffer_load_dwordx4 a[108:111], v226, s[40:43], s58 offen offset:3072 sc1\n"
  "buffer_load_dwordx4 a[112:115], v227, s[40:43], s58 offen offset:0 sc1\n"
  "buffer_load_dwordx4 a[116:119], v227, s[40:43], s58 offen offset:1024 sc1\n"
  "buffer_load_dwordx4 a[120:123], v227, s[40:43], s58 offen offset:2048 sc1\n"
  "buffer_load_dwordx4 a[124:127], v227, s[40:43], s58 offen offset:3072 sc1\n"
  "buffer_load_dwordx2 v[222:223], v235, s[52:55], s57 offen sc1\n"
  "s_cmp_lg_u32 s58, s44\n"
  "s_cbranch_scc1 L_psV_%=\n"
  "L_pbV_%=:\n"
  "L_go_%=:\n"
  "s_waitcnt lgkmcnt(0)\n"
  "L_main_%=:\n"
  "s_nop 1\n"
  "s_waitcnt vmcnt(15)\n"
  "v_mfma_f32_16x16x32_bf16 v[128:131], a[192:195], a[128:131], 0\n"
  "v_mfma_f32_16x16x32_bf16 v[128:131], a[196:199], a[132:135], v[128:131]\n"
  "s_add_u32 s48, s48, 0x4000\n"
  "s_waitcnt vmcnt(13)\n"
  "v_mfma_f32_16x16x32_bf16 v[128:131], a[200:203], a[136:139], v[128:131]\n"
  "v_mfma_f32_16x16x32_bf16 v[128:131], a[204:207], a[140:143], v[128:131]\n"
  "s_xor_b32 s44, s48, s56\n"
  "s_waitcnt vmcnt(11)\n"
  "v_mfma_f32_16x16x32_bf16 v[132:135], a[208:211], a[128:131], 0\n"
  "v_mfma_f32_16x16x32_bf16 v[132:135], a[212:215], a[132:135], v[132:135]\n"
  "s_waitcnt vmcnt(9)\n"
  "v_mfma_f32_16x16x32_bf16 v[132:135], a[216:219], a[136:139], v[132:135]\n"
  "s_nop 1\n"
  "v_max3_f32 v212, v212, v128, v129\n"
  "v_max3_f32 v212, v212, v130, v131\n"
  "v_mfma_f32_16x16x32_bf16 v[132:135], a[220:223], a[140:143], v[132:135]\n"
  "v_mfma_f32_16x16x32_bf16 v[136:139], a[192:195], a[144:147], 0\n"
  "buffer_load_dwordx4 a[224:227], v224, s[40:43], s45 offen offset:0 sc1\n"
  "v_mfma_f32_16x16x32_bf16 v[136:139], a[196:199], a[148:151], v[136:139]\n"
  "buffer_load_dwordx4 a[228:231], v224, s[40:43], s45 offen offset:1024 sc1\n"
  "v_mfma_f32_16x16x32_bf16 v[136:139], a[200:203], a[152:155], v[136:139]\n"
  "buffer_load_dwordx4 a[232:235], v224, s[40:43], s45 offen offset:2048 sc1\n"
  "s_nop 1\n"
  "v_max3_f32 v216, v132, v133, v134\n"
  "v_max3_f32 v212, v212, v135, v216\n"
  "v_mov_b32_e32 v216, v212\n"
  "s_nop 1\n"
  "v_permlane32_swap_b32_e32 v212, v216\n"
  "v_max_f32_e32 v212, v212, v216\n"
  "v_mfma_f32_16x16x32_bf16 v[136:139], a[204:207], a[156:159], v[136:139]\n"
  "buffer_load_dwordx4 a[236:239], v224, s[40:43], s45 offen offset:3072 sc1\n"
  "v_mov_b32_e32 v216, v212\n"
  "s_nop 1\n"
  "v_permlane16_swap_b32_e32 v212, v216\n"
  "v_max_f32_e32 v212, v212, v216\n"
  "v_sub_f32_e32 v128, v128, v212\n"
  "v_sub_f32_e32 v129, v129, v212\n"
  "v_mfma_f32_16x16x32_bf16 v[140:143], a[208:211], a[144:147], 0\n"
  "buffer_load_dwordx4 a[240:243], v225, s[40:43], s45 offen offset:0 sc1\n"
  "v_sub_f32_e32 v130, v130, v212\n"
  "v_sub_f32_e32 v131, v131, v212\n"
  "v_sub_f32_e32 v132, v132, v212\n"
  "v_sub_f32_e32 v133, v133, v212\n"
  "v_sub_f32_e32 v134, v134, v212\n"
  "v_mfma_f32_16x16x32_bf16 v[140:143], a[212:215], a[148:151], v[140:143]\n"
  "buffer_load_dwordx4 a[244:247], v225, s[40:43], s45 offen offset:1024 sc1\n"
  "v_sub_f32_e32 v135, v135, v212\n"
  "v_exp_f32_e32 v128, v128\n"
  "v_exp_f32_e32 v129, v129\n"
  "v_mfma_f32_16x16x32_bf16 v[140:143], a[216:219], a[152:155], v[140:143]\n"
  "buffer_load_dwordx4 a[248:251], v225, s[40:43], s45 offen offset:2048 sc1\n"
  "v_exp_f32_e32 v130, v130\n"
  "v_cvt_pk_bf16_f32 v160, v128, v129\n"
  "v_exp_f32_e32 v131, v131\n"
  "v_mfma_f32_16x16x32_bf16 v[140:143], a[220:223], a[156:159], v[140:143]\n"
  "buffer_load_dwordx4 a[252:255], v225, s[40:43], s45 offen offset:3072 sc1\n"
  "v_cvt_pk_bf16_f32 v161, v130, v131\n"
  "v_sub_f32_e32 v176, 0, v212\n"
  "v_sub_f32_e32 v177, 0, v212\n"
  "v_sub_f32_e32 v178, 0, v212\n"
  "v_sub_f32_e32 v179, 0, v212\n"
  "v_mfma_f32_16x16x32_bf16 v[144:147], a[192:195], a[160:163], 0\n"
  "buffer_load_dwordx4 a[64:67], v226, s[40:43], s45 offen offset:0 sc1\n"
  "v_exp_f32_e32 v132, v132\n"
  "v_exp_f32_e32 v133, v133\n"
  "v_mfma_f32_16x16x32_bf16 v[144:147], a[196:199], a[164:167], v[144:147]\n"
  "v_exp_f32_e32 v134, v134\n"
  "v_cvt_pk_bf16_f32 v162, v132, v133\n"
  "v_exp_f32_e32 v135, v135\n"
  "s_nop 0\n"
  "v_cvt_pk_bf16_f32 v163, v134, v135\n"
  "v_mfma_f32_16x16x32_bf16 v[144:147], a[200:203], a[168:171], v[144:147]\n"
  "buffer_load_dwordx4 a[68:71], v226, s[40:43], s45 offen offset:1024 sc1\n"
  "v_max3_f32 v213, v213, v136, v137\n"
  "v_max3_f32 v213, v213, v138, v139\n"
  "v_max3_f32 v217, v140, v141, v142\n"
  "v_max3_f32 v213, v213, v143, v217\n"
  "v_mov_b32_e32 v217, v213\n"
  "v_mfma_f32_16x16x32_bf16 v[144:147], a[204:207], a[172:175], v[144:147]\n"
  "s_nop 0\n"
  "v_permlane32_swap_b32_e32 v213, v217\n"
  "v_max_f32_e32 v213, v213, v217\n"
  "v_mov_b32_e32 v217, v213\n"
  "s_nop 1\n"
  "v_permlane16_swap_b32_e32 v213, v217\n"
  "v_max_f32_e32 v213, v213, v217\n"
  "v_sub_f32_e32 v136, v136, v213\n"
  "v_mfma_f32_16x16x32_bf16 v[148:151], a[208:211], a[160:163], 0\n"
  "buffer_load_dwordx4 a[72:75], v226, s[40:43], s45 offen offset:2048 sc1\n"
  "v_sub_f32_e32 v137, v137, v213\n"
  "v_sub_f32_e32 v138, v138, v213\n"
  "v_sub_f32_e32 v139, v139, v213\n"
  "v_sub_f32_e32 v140, v140, v213\n"
  "v_sub_f32_e32 v141, v141, v213\n"
  "v_mfma_f32_16x16x32_bf16 v[148:151], a[212:215], a[164:167], v[148:151]\n"
  "v_sub_f32_e32 v142, v142, v213\n"
  "v_sub_f32_e32 v143, v143, v213\n"
  "v_exp_f32_e32 v136, v136\n"
  "v_exp_f32_e32 v137, v137\n"
  "v_mfma_f32_16x16x32_bf16 v[148:151], a[216:219], a[168:171], v[148:151]\n"
  "buffer_load_dwordx4 a[76:79], v226, s[40:43], s45 offen offset:3072 sc1\n"
  "v_exp_f32_e32 v138, v138\n"
  "v_cvt_pk_bf16_f32 v164, v136, v137\n"
  "v_exp_f32_e32 v139, v139\n"
  "v_mfma_f32_16x16x32_bf16 v[148:151], a[220:223], a[172:175], v[148:151]\n"
  "v_cvt_pk_bf16_f32 v165, v138, v139\n"
  "v_sub_f32_e32 v180, 0, v213\n"
  "v_sub_f32_e32 v181, 0, v213\n"
  "v_sub_f32_e32 v182, 0, v213\n"
  "v_sub_f32_e32 v183, 0, v213\n"
  "v_mfma_f32_16x16x32_bf16 v[152:155], a[192:195], a[176:179], 0\n"
  "buffer_load_dwordx4 a[80:83], v227, s[40:43], s45 offen offset:0 sc1\n"
  "v_exp_f32_e32 v140, v140\n"
  "v_exp_f32_e32 v141, v141\n"
  "v_mfma_f32_16x16x32_bf16 v[152:155], a[196:199], a[180:183], v[152:155]\n"
  "v_exp_f32_e32 v142, v142\n"
  "v_cvt_pk_bf16_f32 v166, v140, v141\n"
  "v_exp_f32_e32 v143, v143\n"
  "s_nop 0\n"
  "v_cvt_pk_bf16_f32 v167, v142, v143\n"
  "v_mfma_f32_16x16x32_bf16 v[152:155], a[200:203], a[184:187], v[152:155]\n"
  "buffer_load_dwordx4 a[84:87], v227, s[40:43], s45 offen offset:1024 sc1\n"
  "v_max3_f32 v214, v214, v144, v145\n"
  "v_max3_f32 v214, v214, v146, v147\n"
  "v_max3_f32 v218, v148, v149, v150\n"
  "v_max3_f32 v214, v214, v151, v218\n"
  "v_mov_b32_e32 v218, v214\n"
  "v_mfma_f32_16x16x32_bf16 v[152:155], a[204:207], a[188:191], v[152:155]\n"
  "s_nop 0\n"
  "v_permlane32_swap_b32_e32 v214, v218\n"
  "v_max_f32_e32 v214, v214, v218\n"
  "v_mov_b32_e32 v218, v214\n"
  "s_nop 1\n"
  "v_permlane16_swap_b32_e32 v214, v218\n"
  "v_max_f32_e32 v214, v214, v218\n"
  "v_sub_f32_e32 v144, v144, v214\n"
  "v_mfma_f32_16x16x32_bf16 v[156:159], a[208:211], a[176:179], 0\n"
  "buffer_load_dwordx4 a[88:91], v227, s[40:43], s45 offen offset:2048 sc1\n"
  "v_sub_f32_e32 v145, v145, v214\n"
  "v_sub_f32_e32 v146, v146, v214\n"
  "v_sub_f32_e32 v147, v147, v214\n"
  "v_sub_f32_e32 v148, v148, v214\n"
  "v_sub_f32_e32 v149, v149, v214\n"
  "v_mfma_f32_16x16x32_bf16 v[156:159], a[212:215], a[180:183], v[156:159]\n"
  "v_sub_f32_e32 v150, v150, v214\n"
  "v_sub_f32_e32 v151, v151, v214\n"
  "v_exp_f32_e32 v144, v144\n"
  "v_exp_f32_e32 v145, v145\n"
  "v_mfma_f32_16x16x32_bf16 v[156:159], a[216:219], a[184:187], v[156:159]\n"
  "buffer_load_dwordx4 a[92:95], v227, s[40:43], s45 offen offset:3072 sc1\n"
  "s_waitcnt vmcnt(16)\n"
  "v_cmp_ne_u64_e32 vcc, s[50:51], v[222:223]\n"
  "v_exp_f32_e32 v146, v146\n"
  "v_cvt_pk_bf16_f32 v168, v144, v145\n"
  "v_mfma_f32_16x16x32_bf16 v[156:159], a[220:223], a[188:191], v[156:159]\n"
  "v_exp_f32_e32 v147, v147\n"
  "s_nop 0\n"
  "v_cvt_pk_bf16_f32 v169, v146, v147\n"
  "v_sub_f32_e32 v184, 0, v214\n"
  "v_sub_f32_e32 v185, 0, v214\n"
  "v_sub_f32_e32 v186, 0, v214\n"
  "s_waitcnt vmcnt(14)\n"
  "v_mfma_f32_16x16x32_bf16 v[128:131], a[224:227], a[128:131], v[176:179]\n"
  "s_cbranch_vccnz L_spin0_%=\n"
  "L_spin0back_%=:\n"
  "v_sub_f32_e32 v187, 0, v214\n"
  "v_max3_f32 v215, v215, v152, v153\n"
  "v_max3_f32 v215, v215, v154, v155\n"
  "v_mfma_f32_16x16x32_bf16 v[128:131], a[228:231], a[132:135], v[128:131]\n"
  "s_waitcnt vmcnt(12)\n"
  "v_mfma_f32_16x16x32_bf16 v[128:131], a[232:235], a[136:139], v[128:131]\n"
  "buffer_load_dwordx4 a[192:195], v224, s[40:43], s44 offen offset:0 sc1\n"
  "v_max3_f32 v219, v156, v157, v158\n"
  "v_max3_f32 v215, v215, v159, v219\n"
  "v_mov_b32_e32 v219, v215\n"
  "s_nop 1\n"
  "v_permlane32_swap_b32_e32 v215, v219\n"
  "v_max_f32_e32 v215, v215, v219\n"
  "v_mfma_f32_16x16x32_bf16 v[128:131], a[236:239], a[140:143], v[128:131]\n"
  "s_and_b32 s57, s46, 0xf00\n"
  "v_mov_b32_e32 v219, v215\n"
  "s_nop 1\n"
  "v_permlane16_swap_b32_e32 v215, v219\n"
  "v_max_f32_e32 v215, v215, v219\n"
  "v_sub_f32_e32 v152, v152, v215\n"
  "v_sub_f32_e32 v153, v153, v215\n"
  "v_mfma_f32_16x16x32_bf16 v[136:139], a[224:227], a[144:147], v[180:183]\n"
  "buffer_load_dwordx4 a[196:199], v224, s[40:43], s44 offen offset:1024 sc1\n"
  "v_sub_f32_e32 v154, v154, v215\n"
  "v_sub_f32_e32 v155, v155, v215\n"
  "v_sub_f32_e32 v156, v156, v215\n"
  "v_sub_f32_e32 v157, v157, v215\n"
  "v_sub_f32_e32 v158, v158, v215\n"
  "v_mfma_f32_16x16x32_bf16 v[136:139], a[228:231], a[148:151], v[136:139]\n"
  "v_sub_f32_e32 v159, v159, v215\n"
  "v_exp_f32_e32 v152, v152\n"
  "v_exp_f32_e32 v153, v153\n"
  "v_mfma_f32_16x16x32_bf16 v[136:139], a[232:235], a[152:155], v[136:139]\n"
  "buffer_load_dwordx4 a[200:203], v224, s[40:43], s44 offen offset:2048 sc1\n"
  "v_exp_f32_e32 v154, v154\n"
  "v_cvt_pk_bf16_f32 v172, v152, v153\n"
  "v_exp_f32_e32 v155, v155\n"
  "v_mfma_f32_16x16x32_bf16 v[136:139], a[236:239], a[156:159], v[136:139]\n"
  "v_exp_f32_e32 v128, v128\n"
  "v_cvt_pk_bf16_f32 v173, v154, v155\n"
  "v_sub_f32_e32 v188, 0, v215\n"
  "v_sub_f32_e32 v189, 0, v215\n"
  "v_sub_f32_e32 v190, 0, v215\n"
  "v_mfma_f32_16x16x32_bf16 v[144:147], a[224:227], a[160:163], v[184:187]\n"
  "buffer_load_dwordx4 a[204:207], v224, s[40:43], s44 offen offset:3072 sc1\n"
  "v_sub_f32_e32 v191, 0, v215\n"
  "v_exp_f32_e32 v148, v148\n"
  "v_exp_f32_e32 v149, v149\n"
  "v_mfma_f32_16x16x32_bf16 v[144:147], a[228:231], a[164:167], v[144:147]\n"
  "v_exp_f32_e32 v129, v129\n"
  "v_exp_f32_e32 v150, v150\n"
  "v_cvt_pk_bf16_f32 v170, v148, v149\n"
  "v_mfma_f32_16x16x32_bf16 v[144:147], a[232:235], a[168:171], v[144:147]\n"
  "buffer_load_dwordx4 a[208:211], v225, s[40:43], s44 offen offset:0 sc1\n"
  "v_cvt_pk_bf16_f32 v24, v128, v129\n"
  "v_exp_f32_e32 v151, v151\n"
  "s_nop 0\n"
  "v_cvt_pk_bf16_f32 v171, v150, v151\n"
  "v_mfma_f32_16x16x32_bf16 v[144:147], a[236:239], a[172:175], v[144:147]\n"
  "v_exp_f32_e32 v130, v130\n"
  "v_exp_f32_e32 v156, v156\n"
  "v_exp_f32_e32 v157, v157\n"
  "v_mfma_f32_16x16x32_bf16 v[152:155], a[224:227], a[176:179], v[188:191]\n"
  "buffer_load_dwordx4 a[212:215], v225, s[40:43], s44 offen offset:1024 sc1\n"
  "v_exp_f32_e32 v158, v158\n"
  "v_cvt_pk_bf16_f32 v174, v156, v157\n"
  "v_exp_f32_e32 v159, v159\n"
  "v_mfma_f32_16x16x32_bf16 v[152:155], a[228:231], a[180:183], v[152:155]\n"
  "v_exp_f32_e32 v131, v131\n"
  "v_cvt_pk_bf16_f32 v175, v158, v159\n"
  "v_mfma_f32_16x16x32_bf16 v[152:155], a[232:235], a[184:187], v[152:155]\n"
  "buffer_load_dwordx4 a[216:219], v225, s[40:43], s44 offen offset:2048 sc1\n"
  "v_cvt_pk_bf16_f32 v25, v130, v131\n"
  "v_mfma_f32_16x16x32_bf16 v[152:155], a[236:239], a[188:191], v[152:155]\n"
  "v_exp_f32_e32 v136, v136\n"
  "v_mfma_f32_16x16x32_bf16 v[228:231], a[96:99], v[160:163], 0\n"
  "buffer_load_dwordx4 a[220:223], v225, s[40:43], s44 offen offset:3072 sc1\n"
  "v_mfma_f32_16x16x32_bf16 v[4:7], a[96:99], v[164:167], 0\n"
  "v_exp_f32_e32 v137, v137\n"
  "v_mfma_f32_16x16x32_bf16 v[236:239], a[100:103], v[160:163], 0\n"
  "v_cvt_pk_bf16_f32 v28, v136, v137\n"
  "buffer_store_dwordx4 v[8:11], v2, s[36:39], 0 offen offset:0 sc1\n"
  "v_mfma_f32_16x16x32_bf16 v[40:43], a[100:103], v[164:167], 0\n"
  "v_exp_f32_e32 v138, v138\n"
  "v_mfma_f32_16x16x32_bf16 v[240:243], a[104:107], v[160:163], 0\n"
  "buffer_store_dwordx4 v[12:15], v2, s[36:39], 0 offen offset:2048 sc1\n"
  "v_mfma_f32_16x16x32_bf16 v[44:47], a[104:107], v[164:167], 0\n"
  "v_exp_f32_e32 v139, v139\n"
  "v_mfma_f32_16x16x32_bf16 v[204:207], a[108:111], v[160:163], 0\n"
  "v_cvt_pk_bf16_f32 v29, v138, v139\n"
  "buffer_store_dwordx4 v[16:19], v3, s[36:39], 0 offen offset:0 sc1\n"
  "v_mfma_f32_16x16x32_bf16 v[48:51], a[108:111], v[164:167], 0\n"
  "v_exp_f32_e32 v144, v144\n"
  "v_mfma_f32_16x16x32_bf16 v[96:99], a[96:99], v[168:171], 0\n"
  "buffer_store_dwordx4 v[20:23], v3, s[36:39], 0 offen offset:2048 sc1\n"
  "v_mfma_f32_16x16x32_bf16 v[64:67], v[172:175], a[96:99], 0\n"
  "v_exp_f32_e32 v145, v145\n"
  "v_mfma_f32_16x16x32_bf16 v[100:103], a[100:103], v[168:171], 0\n"
  "v_cvt_pk_bf16_f32 v32, v144, v145\n"
  "v_mfma_f32_16x16x32_bf16 v[68:71], v[172:175], a[100:103], 0\n"
  "v_exp_f32_e32 v146, v146\n"
  "v_mfma_f32_16x16x32_bf16 v[104:107], a[104:107], v[168:171], 0\n"
  "v_mfma_f32_16x16x32_bf16 v[72:75], v[172:175], a[104:107], 0\n"
  "v_exp_f32_e32 v147, v147\n"
  "v_mfma_f32_16x16x32_bf16 v[108:111], a[108:111], v[168:171], 0\n"
  "v_cvt_pk_bf16_f32 v33, v146, v147\n"
  "v_mfma_f32_16x16x32_bf16 v[76:79], v[172:175], a[108:111], 0\n"
  "v_exp_f32_e32 v152, v152\n"
  "v_smfmac_f32_16x16x64_bf16 v[192:195], v[208:211], v[160:167], v247\n"
  "s_waitcnt vmcnt(22)\n"
  "v_mfma_f32_16x16x32_bf16 v[132:135], a[240:243], a[128:131], v[176:179]\n"
  "v_exp_f32_e32 v153, v153\n"
  "v_mfma_f32_16x16x32_bf16 v[132:135], a[244:247], a[132:135], v[132:135]\n"
  "v_cvt_pk_bf16_f32 v36, v152, v153\n"
  "s_add_u32 s48, s48, 0x4000\n"
  "s_waitcnt vmcnt(20)\n"
  "v_mfma_f32_16x16x32_bf16 v[132:135], a[248:251], a[136:139], v[132:135]\n"
  "v_exp_f32_e32 v154, v154\n"
  "v_mfma_f32_16x16x32_bf16 v[132:135], a[252:255], a[140:143], v[132:135]\n"
  "v_mfma_f32_16x16x32_bf16 v[140:143], a[240:243], a[144:147], v[180:183]\n"
  "v_exp_f32_e32 v155, v155\n"
  "v_mfma_f32_16x16x32_bf16 v[140:143], a[244:247], a[148:151], v[140:143]\n"
  "v_cvt_pk_bf16_f32 v37, v154, v155\n"
  "v_mfma_f32_16x16x32_bf16 v[140:143], a[248:251], a[152:155], v[140:143]\n"
  "buffer_load_dwordx2 v[222:223], v235, s[52:55], s57 offen sc1\n"
  "v_mfma_f32_16x16x32_bf16 v[140:143], a[252:255], a[156:159], v[140:143]\n"
  "s_nop 0\n"
  "v_exp_f32_e32 v132, v132\n"
  "v_mfma_f32_16x16x32_bf16 v[148:151], a[240:243], a[160:163], v[184:187]\n"
  "v_mfma_f32_16x16x32_bf16 v[148:151], a[244:247], a[164:167], v[148:151]\n"
  "v_exp_f32_e32 v133, v133\n"
  "v_mfma_f32_16x16x32_bf16 v[148:151], a[248:251], a[168:171], v[148:151]\n"
  "v_cvt_pk_bf16_f32 v26, v132, v133\n"
  "v_mfma_f32_16x16x32_bf16 v[148:151], a[252:255], a[172:175], v[148:151]\n"
  "v_exp_f32_e32 v134, v134\n"
  "v_mfma_f32_16x16x32_bf16 v[156:159], a[240:243], a[176:179], v[188:191]\n"
  "v_mfma_f32_16x16x32_bf16 v[156:159], a[244:247], a[180:183], v[156:159]\n"
  "v_exp_f32_e32 v135, v135\n"
  "v_mfma_f32_16x16x32_bf16 v[156:159], a[248:251], a[184:187], v[156:159]\n"
  "v_cvt_pk_bf16_f32 v27, v134, v135\n"
  "v_mfma_f32_16x16x32_bf16 v[156:159], a[252:255], a[188:191], v[156:159]\n"
  "v_exp_f32_e32 v140, v140\n"
  "v_mfma_f32_16x16x32_bf16 v[8:11], a[112:115], v[160:163], 0\n"
  "v_mfma_f32_16x16x32_bf16 v[52:55], a[112:115], v[164:167], 0\n"
  "v_exp_f32_e32 v141, v141\n"
  "v_mfma_f32_16x16x32_bf16 v[12:15], a[116:119], v[160:163], 0\n"
  "v_cvt_pk_bf16_f32 v30, v140, v141\n"
  "v_mfma_f32_16x16x32_bf16 v[56:59], a[116:119], v[164:167], 0\n"
  "v_exp_f32_e32 v142, v142\n"
  "v_mfma_f32_16x16x32_bf16 v[16:19], a[120:123], v[160:163], 0\n"
  "v_mfma_f32_16x16x32_bf16 v[60:63], a[120:123], v[164:167], 0\n"
  "v_exp_f32_e32 v143, v143\n"
  "v_mfma_f32_16x16x32_bf16 v[20:23], a[124:127], v[160:163], 0\n"
  "v_cvt_pk_bf16_f32 v31, v142, v143\n"
  "v_mfma_f32_16x16x32_bf16 v[196:199], a[124:127], v[164:167], 0\n"
  "v_exp_f32_e32 v148, v148\n"
  "v_mfma_f32_16x16x32_bf16 v[112:115], a[112:115], v[168:171], 0\n"
  "v_mfma_f32_16x16x32_bf16 v[80:83], v[172:175], a[112:115], 0\n"
  "v_exp_f32_e32 v149, v149\n"
  "v_mfma_f32_16x16x32_bf16 v[116:119], a[116:119], v[168:171], 0\n"
  "v_cvt_pk_bf16_f32 v34, v148, v149\n"
  "v_mfma_f32_16x16x32_bf16 v[84:87], v[172:175], a[116:119], 0\n"
  "v_exp_f32_e32 v150, v150\n"
  "v_mfma_f32_16x16x32_bf16 v[120:123], a[120:123], v[168:171], 0\n"
  "v_mfma_f32_16x16x32_bf16 v[88:91], v[172:175], a[120:123], 0\n"
  "v_exp_f32_e32 v151, v151\n"
  "v_mfma_f32_16x16x32_bf16 v[124:127], a[124:127], v[168:171], 0\n"
  "v_cvt_pk_bf16_f32 v35, v150, v151\n"
  "s_waitcnt vmcnt(9)\n"
  "v_mfma_f32_16x16x32_bf16 v[92:95], v[172:175], a[124:127], 0\n"
  "v_exp_f32_e32 v156, v156\n"
  "v_smfmac_f32_16x16x64_bf16 v[200:203], v[208:211], v[168:175], v247\n"
  "v_mfma_f32_16x16x32_bf16 v[128:131], a[192:195], a[128:131], v[176:179]\n"
  "v_exp_f32_e32 v157, v157\n"
  "v_mfma_f32_16x16x32_bf16 v[128:131], a[196:199], a[132:135], v[128:131]\n"
  "v_cvt_pk_bf16_f32 v38, v156, v157\n"
  "v_mfma_f32_16x16x32_bf16 v[128:131], a[200:203], a[136:139], v[128:131]\n"
  "v_exp_f32_e32 v158, v158\n"
  "v_mfma_f32_16x16x32_bf16 v[128:131], a[204:207], a[140:143], v[128:131]\n"
  "v_mfma_f32_16x16x32_bf16 v[136:139], a[192:195], a[144:147], v[180:183]\n"
  "v_exp_f32_e32 v159, v159\n"
  "v_mfma_f32_16x16x32_bf16 v[136:139], a[196:199], a[148:151], v[136:139]\n"
  "v_cvt_pk_bf16_f32 v39, v158, v159\n"
  "s_xor_b32 s45, s48, s56\n"
  "v_mfma_f32_16x16x32_bf16 v[136:139], a[200:203], a[152:155], v[136:139]\n"
  "s_add_u32 s46, s46, 0x80\n"
  "v_mfma_f32_16x16x32_bf16 v[136:139], a[204:207], a[156:159], v[136:139]\n"
  "v_exp_f32_e32 v128, v128\n"
  "v_mfma_f32_16x16x32_bf16 v[144:147], a[192:195], a[160:163], v[184:187]\n"
  "buffer_load_dwordx4 a[224:227], v224, s[40:43], s45 offen offset:0 sc1\n"
  "v_mfma_f32_16x16x32_bf16 v[144:147], a[196:199], a[164:167], v[144:147]\n"
  "v_exp_f32_e32 v129, v129\n"
  "v_mfma_f32_16x16x32_bf16 v[144:147], a[200:203], a[168:171], v[144:147]\n"
  "v_cvt_pk_bf16_f32 v160, v128, v129\n"
  "v_mfma_f32_16x16x32_bf16 v[144:147], a[204:207], a[172:175], v[144:147]\n"
  "v_exp_f32_e32 v130, v130\n"
  "v_mfma_f32_16x16x32_bf16 v[152:155], a[192:195], a[176:179], v[188:191]\n"
  "buffer_load_dwordx4 a[228:231], v224, s[40:43], s45 offen offset:1024 sc1\n"
  "v_mfma_f32_16x16x32_bf16 v[152:155], a[196:199], a[180:183], v[152:155]\n"
  "v_exp_f32_e32 v131, v131\n"
  "v_mfma_f32_16x16x32_bf16 v[152:155], a[200:203], a[184:187], v[152:155]\n"
  "v_cvt_pk_bf16_f32 v161, v130, v131\n"
  "v_mfma_f32_16x16x32_bf16 v[152:155], a[204:207], a[188:191], v[152:155]\n"
  "v_exp_f32_e32 v136, v136\n"
  "v_mfma_f32_16x16x32_bf16 v[228:231], a[64:67], v[24:27], v[228:231]\n"
  "buffer_load_dwordx4 a[232:235], v224, s[40:43], s45 offen offset:2048 sc1\n"
  "v_mfma_f32_16x16x32_bf16 v[4:7], a[64:67], v[28:31], v[4:7]\n"
  "v_exp_f32_e32 v137, v137\n"
  "v_mfma_f32_16x16x32_bf16 v[236:239], a[68:71], v[24:27], v[236:239]\n"
  "v_cvt_pk_bf16_f32 v164, v136, v137\n"
  "v_mfma_f32_16x16x32_bf16 v[40:43], a[68:71], v[28:31], v[40:43]\n"
  "v_exp_f32_e32 v138, v138\n"
  "v_mfma_f32_16x16x32_bf16 v[240:243], a[72:75], v[24:27], v[240:243]\n"
  "buffer_load_dwordx4 a[236:239], v224, s[40:43], s45 offen offset:3072 sc1\n"
  "v_mfma_f32_16x16x32_bf16 v[44:47], a[72:75], v[28:31], v[44:47]\n"
  "v_exp_f32_e32 v139, v139\n"
  "v_mfma_f32_16x16x32_bf16 v[204:207], a[76:79], v[24:27], v[204:207]\n"
  "v_cvt_pk_bf16_f32 v165, v138, v139\n"
  "v_mfma_f32_16x16x32_bf16 v[48:51], a[76:79], v[28:31], v[48:51]\n"
  "v_exp_f32_e32 v144, v144\n"
  "v_mfma_f32_16x16x32_bf16 v[96:99], a[64:67], v[32:35], v[96:99]\n"
  "buffer_load_dwordx4 a[96:99], v226, s[40:43], s44 offen offset:0 sc1\n"
  "v_mfma_f32_16x16x32_bf16 v[64:67], v[36:39], a[64:67], v[64:67]\n"
  "v_exp_f32_e32 v145, v145\n"
  "v_mfma_f32_16x16x32_bf16 v[100:103], a[68:71], v[32:35], v[100:103]\n"
  "v_cvt_pk_bf16_f32 v168, v144, v145\n"
  "v_mfma_f32_16x16x32_bf16 v[68:71], v[36:39], a[68:71], v[68:71]\n"
  "v_exp_f32_e32 v146, v146\n"
  "v_mfma_f32_16x16x32_bf16 v[104:107], a[72:75], v[32:35], v[104:107]\n"
  "buffer_load_dwordx4 a[100:103], v226, s[40:43], s44 offen offset:1024 sc1\n"
  "v_mfma_f32_16x16x32_bf16 v[72:75], v[36:39], a[72:75], v[72:75]\n"
  "v_exp_f32_e32 v147, v147\n"
  "v_mfma_f32_16x16x32_bf16 v[108:111], a[76:79], v[32:35], v[108:111]\n"
  "v_cvt_pk_bf16_f32 v169, v146, v147\n"
  "s_waitcnt vmcnt(11)\n"
  "v_mfma_f32_16x16x32_bf16 v[76:79], v[36:39], a[76:79], v[76:79]\n"
  "v_exp_f32_e32 v152, v152\n"
  "v_smfmac_f32_16x16x64_bf16 v[192:195], v[208:211], v[24:31], v247\n"
  "buffer_load_dwordx4 a[104:107], v226, s[40:43], s44 offen offset:2048 sc1\n"
  "v_mfma_f32_16x16x32_bf16 v[132:135], a[208:211], a[128:131], v[176:179]\n"
  "v_exp_f32_e32 v153, v153\n"
  "v_mfma_f32_16x16x32_bf16 v[132:135], a[212:215], a[132:135], v[132:135]\n"
  "v_cvt_pk_bf16_f32 v172, v152, v153\n"
  "s_add_u32 s48, s48, 0x4000\n"
  "v_mfma_f32_16x16x32_bf16 v[132:135], a[216:219], a[136:139], v[132:135]\n"
  "v_exp_f32_e32 v154, v154\n"
  "v_mfma_f32_16x16x32_bf16 v[132:135], a[220:223], a[140:143], v[132:135]\n"
  "buffer_load_dwordx4 a[108:111], v226, s[40:43], s44 offen offset:3072 sc1\n"
  "v_mfma_f32_16x16x32_bf16 v[140:143], a[208:211], a[144:147], v[180:183]\n"
  "v_exp_f32_e32 v155, v155\n"
  "v_mfma_f32_16x16x32_bf16 v[140:143], a[212:215], a[148:151], v[140:143]\n"
  "v_cvt_pk_bf16_f32 v173, v154, v155\n"
  "v_mfma_f32_16x16x32_bf16 v[140:143], a[216:219], a[152:155], v[140:143]\n"
  "s_waitcnt vmcnt(8)\n"
  "v_cmp_ne_u64_e32 vcc, s[50:51], v[222:223]\n"
  "v_mfma_f32_16x16x32_bf16 v[140:143], a[220:223], a[156:159], v[140:143]\n"
  "v_exp_f32_e32 v132, v132\n"
  "v_mfma_f32_16x16x32_bf16 v[148:151], a[208:211], a[160:163], v[184:187]\n"
  "buffer_load_dwordx4 a[240:243], v225, s[40:43], s45 offen offset:0 sc1\n"
  "v_mfma_f32_16x16x32_bf16 v[148:151], a[212:215], a[164:167], v[148:151]\n"
  "v_exp_f32_e32 v133, v133\n"
  "v_mfma_f32_16x16x32_bf16 v[148:151], a[216:219], a[168:171], v[148:151]\n"
  "v_cvt_pk_bf16_f32 v162, v132, v133\n"
  "s_cbranch_vccnz L_spin2_%=\n"
  "L_spin2back_%=:\n"
  "v_mfma_f32_16x16x32_bf16 v[148:151], a[220:223], a[172:175], v[148:151]\n"
  "v_exp_f32_e32 v134, v134\n"
  "v_mfma_f32_16x16x32_bf16 v[156:159], a[208:211], a[176:179], v[188:191]\n"
  "buffer_load_dwordx4 a[244:247], v225, s[40:43], s45 offen offset:1024 sc1\n"
  "v_mfma_f32_16x16x32_bf16 v[156:159], a[212:215], a[180:183], v[156:159]\n"
  "v_exp_f32_e32 v135, v135\n"
  "v_mfma_f32_16x16x32_bf16 v[156:159], a[216:219], a[184:187], v[156:159]\n"
  "v_cvt_pk_bf16_f32 v163, v134, v135\n"
  "v_mfma_f32_16x16x32_bf16 v[156:159], a[220:223], a[188:191], v[156:159]\n"
  "v_exp_f32_e32 v140, v140\n"
  "v_mfma_f32_16x16x32_bf16 v[8:11], a[80:83], v[24:27], v[8:11]\n"
  "buffer_load_dwordx4 a[248:251], v225, s[40:43], s45 offen offset:2048 sc1\n"
  "v_mfma_f32_16x16x32_bf16 v[52:55], a[80:83], v[28:31], v[52:55]\n"
  "v_exp_f32_e32 v141, v141\n"
  "v_mfma_f32_16x16x32_bf16 v[12:15], a[84:87], v[24:27], v[12:15]\n"
  "v_cvt_pk_bf16_f32 v166, v140, v141\n"
  "v_mfma_f32_16x16x32_bf16 v[56:59], a[84:87], v[28:31], v[56:59]\n"
  "v_exp_f32_e32 v142, v142\n"
  "v_mfma_f32_16x16x32_bf16 v[16:19], a[88:91], v[24:27], v[16:19]\n"
  "buffer_load_dwordx4 a[252:255], v225, s[40:43], s45 offen offset:3072 sc1\n"
  "v_mfma_f32_16x16x32_bf16 v[60:63], a[88:91], v[28:31], v[60:63]\n"
  "v_exp_f32_e32 v143, v143\n"
  "v_mfma_f32_16x16x32_bf16 v[20:23], a[92:95], v[24:27], v[20:23]\n"
  "v_cvt_pk_bf16_f32 v167, v142, v143\n"
  "v_mfma_f32_16x16x32_bf16 v[196:199], a[92:95], v[28:31], v[196:199]\n"
  "v_exp_f32_e32 v148, v148\n"
  "v_mfma_f32_16x16x32_bf16 v[112:115], a[80:83], v[32:35], v[112:115]\n"
  "buffer_load_dwordx4 a[112:115], v227, s[40:43], s44 offen offset:0 sc1\n"
  "v_mfma_f32_16x16x32_bf16 v[80:83], v[36:39], a[80:83], v[80:83]\n"
  "v_exp_f32_e32 v149, v149\n"
  "v_mfma_f32_16x16x32_bf16 v[116:119], a[84:87], v[32:35], v[116:119]\n"
  "v_cvt_pk_bf16_f32 v170, v148, v149\n"
  "v_mfma_f32_16x16x32_bf16 v[84:87], v[36:39], a[84:87], v[84:87]\n"
  "v_exp_f32_e32 v150, v150\n"
  "v_mfma_f32_16x16x32_bf16 v[120:123], a[88:91], v[32:35], v[120:123]\n"
  "buffer_load_dwordx4 a[116:119], v227, s[40:43], s44 offen offset:1024 sc1\n"
  "v_mfma_f32_16x16x32_bf16 v[88:91], v[36:39], a[88:91], v[88:91]\n"
  "v_exp_f32_e32 v151, v151\n"
  "v_mfma_f32_16x16x32_bf16 v[124:127], a[92:95], v[32:35], v[124:127]\n"
  "v_cvt_pk_bf16_f32 v171, v150, v151\n"
  "s_waitcnt vmcnt(10)\n"
  "v_mfma_f32_16x16x32_bf16 v[92:95], v[36:39], a[92:95], v[92:95]\n"
  "v_exp_f32_e32 v156, v156\n"
  "v_smfmac_f32_16x16x64_bf16 v[200:203], v[208:211], v[32:39], v247\n"
  "buffer_load_dwordx4 a[120:123], v227, s[40:43], s44 offen offset:2048 sc1\n"
  "v_mfma_f32_16x16x32_bf16 v[128:131], a[224:227], a[128:131], v[176:179]\n"
  "v_exp_f32_e32 v157, v157\n"
  "v_mfma_f32_16x16x32_bf16 v[128:131], a[228:231], a[132:135], v[128:131]\n"
  "v_cvt_pk_bf16_f32 v174, v156, v157\n"
  "v_mfma_f32_16x16x32_bf16 v[128:131], a[232:235], a[136:139], v[128:131]\n"
  "v_exp_f32_e32 v158, v158\n"
  "v_mfma_f32_16x16x32_bf16 v[128:131], a[236:239], a[140:143], v[128:131]\n"
  "buffer_load_dwordx4 a[124:127], v227, s[40:43], s44 offen offset:3072 sc1\n"
  "v_mfma_f32_16x16x32_bf16 v[136:139], a[224:227], a[144:147], v[180:183]\n"
  "v_exp_f32_e32 v159, v159\n"
  "v_mfma_f32_16x16x32_bf16 v[136:139], a[228:231], a[148:151], v[136:139]\n"
  "v_cvt_pk_bf16_f32 v175, v158, v159\n"
  "s_xor_b32 s44, s48, s56\n"
  "v_mfma_f32_16x16x32_bf16 v[136:139], a[232:235], a[152:155], v[136:139]\n"
  "s_and_b32 s57, s46, 0xf00\n"
  "buffer_store_dwordx2 v[248:249], v245, s[36:39], 0 offen sc1\n"
  "v_mfma_f32_16x16x32_bf16 v[136:139], a[236:239], a[156:159], v[136:139]\n"
  "v_exp_f32_e32 v128, v128\n"
  "v_mfma_f32_16x16x32_bf16 v[144:147], a[224:227], a[160:163], v[184:187]\n"
  "buffer_load_dwordx4 a[192:195], v224, s[40:43], s44 offen offset:0 sc1\n"
  "v_mfma_f32_16x16x32_bf16 v[144:147], a[228:231], a[164:167], v[144:147]\n"
  "v_exp_f32_e32 v129, v129\n"
  "v_mfma_f32_16x16x32_bf16 v[144:147], a[232:235], a[168:171], v[144:147]\n"
  "v_cvt_pk_bf16_f32 v24, v128, v129\n"
  "v_mfma_f32_16x16x32_bf16 v[144:147], a[236:239], a[172:175], v[144:147]\n"
  "v_exp_f32_e32 v130, v130\n"
  "v_mfma_f32_16x16x32_bf16 v[152:155], a[224:227], a[176:179], v[188:191]\n"
  "buffer_load_dwordx4 a[196:199], v224, s[40:43], s44 offen offset:1024 sc1\n"
  "v_mfma_f32_16x16x32_bf16 v[152:155], a[228:231], a[180:183], v[152:155]\n"
  "v_exp_f32_e32 v131, v131\n"
  "v_mfma_f32_16x16x32_bf16 v[152:155], a[232:235], a[184:187], v[152:155]\n"
  "v_cvt_pk_bf16_f32 v25, v130, v131\n"
  "s_waitcnt vmcnt(11)\n"
  "v_mfma_f32_16x16x32_bf16 v[152:155], a[236:239], a[188:191], v[152:155]\n"
  "v_exp_f32_e32 v136, v136\n"
  "v_mfma_f32_16x16x32_bf16 v[228:231], a[96:99], v[160:163], v[228:231]\n"
  "buffer_load_dwordx4 a[200:203], v224, s[40:43], s44 offen offset:2048 sc1\n"
  "v_mfma_f32_16x16x32_bf16 v[4:7], a[96:99], v[164:167], v[4:7]\n"
  "v_exp_f32_e32 v137, v137\n"
  "v_mfma_f32_16x16x32_bf16 v[236:239], a[100:103], v[160:163], v[236:239]\n"
  "v_cvt_pk_bf16_f32 v28, v136, v137\n"
  "v_mfma_f32_16x16x32_bf16 v[40:43], a[100:103], v[164:167], v[40:43]\n"
  "v_exp_f32_e32 v138, v138\n"
  "v_mfma_f32_16x16x32_bf16 v[240:243], a[104:107], v[160:163], v[240:243]\n"
  "buffer_load_dwordx4 a[204:207], v224, s[40:43], s44 offen offset:3072 sc1\n"
  "v_mfma_f32_16x16x32_bf16 v[44:47], a[104:107], v[164:167], v[44:47]\n"
  "v_exp_f32_e32 v139, v139\n"
  "v_mfma_f32_16x16x32_bf16 v[204:207], a[108:111], v[160:163], v[204:207]\n"
  "v_cvt_pk_bf16_f32 v29, v138, v139\n"
  "v_mfma_f32_16x16x32_bf16 v[48:51], a[108:111], v[164:167], v[48:51]\n"
  "v_exp_f32_e32 v144, v144\n"
  "v_mfma_f32_16x16x32_bf16 v[96:99], a[96:99], v[168:171], v[96:99]\n"
  "buffer_load_dwordx4 a[64:67], v226, s[40:43], s45 offen offset:0 sc1\n"
  "v_mfma_f32_16x16x32_bf16 v[64:67], v[172:175], a[96:99], v[64:67]\n"
  "v_exp_f32_e32 v145, v145\n"
  "v_mfma_f32_16x16x32_bf16 v[100:103], a[100:103], v[168:171], v[100:103]\n"
  "v_cvt_pk_bf16_f32 v32, v144, v145\n"
  "v_mfma_f32_16x16x32_bf16 v[68:71], v[172:175], a[100:103], v[68:71]\n"
  "v_exp_f32_e32 v146, v146\n"
  "v_mfma_f32_16x16x32_bf16 v[104:107], a[104:107], v[168:171], v[104:107]\n"
  "buffer_load_dwordx4 a[68:71], v226, s[40:43], s45 offen offset:1024 sc1\n"
  "v_mfma_f32_16x16x32_bf16 v[72:75], v[172:175], a[104:107], v[72:75]\n"
  "v_exp_f32_e32 v147, v147\n"
  "v_mfma_f32_16x16x32_bf16 v[108:111], a[108:111], v[168:171], v[108:111]\n"
  "v_cvt_pk_bf16_f32 v33, v146, v147\n"
  "s_waitcnt vmcnt(11)\n"
  "v_mfma_f32_16x16x32_bf16 v[76:79], v[172:175], a[108:111], v[76:79]\n"
  "v_exp_f32_e32 v152, v152\n"
  "v_smfmac_f32_16x16x64_bf16 v[192:195], v[208:211], v[160:167], v247\n"
  "buffer_load_dwordx4 a[72:75], v226, s[40:43], s45 offen offset:2048 sc1\n"
  "v_mfma_f32_16x16x32_bf16 v[132:135], a[240:243], a[128:131], v[176:179]\n"
  "v_exp_f32_e32 v153, v153\n"
  "v_mfma_f32_16x16x32_bf16 v[132:135], a[244:247], a[132:135], v[132:135]\n"
  "v_cvt_pk_bf16_f32 v36, v152, v153\n"
  "s_add_u32 s48, s48, 0x4000\n"
  "v_mfma_f32_16x16x32_bf16 v[132:135], a[248:251], a[136:139], v[132:135]\n"
  "v_exp_f32_e32 v154, v154\n"
  "v_mfma_f32_16x16x32_bf16 v[132:135], a[252:255], a[140:143], v[132:135]\n"
  "buffer_load_dwordx4 a[76:79], v226, s[40:43], s45 offen offset:3072 sc1\n"
  "v_mfma_f32_16x16x32_bf16 v[140:143], a[240:243], a[144:147], v[180:183]\n"
  "v_exp_f32_e32 v155, v155\n"
  "v_mfma_f32_16x16x32_bf16 v[140:143], a[244:247], a[148:151], v[140:143]\n"
  "v_cvt_pk_bf16_f32 v37, v154, v155\n"
  "v_mfma_f32_16x16x32_bf16 v[140:143], a[248:251], a[152:155], v[140:143]\n"
  "buffer_load_dwordx2 v[222:223], v235, s[52:55], s57 offen sc1\n"
  "v_mfma_f32_16x16x32_bf16 v[140:143], a[252:255], a[156:159], v[140:143]\n"
  "v_exp_f32_e32 v132, v132\n"
  "v_mfma_f32_16x16x32_bf16 v[148:151], a[240:243], a[160:163], v[184:187]\n"
  "buffer_load_dwordx4 a[208:211], v225, s[40:43], s44 offen offset:0 sc1\n"
  "v_mfma_f32_16x16x32_bf16 v[148:151], a[244:247], a[164:167], v[148:151]\n"
  "v_exp_f32_e32 v133, v133\n"
  "v_mfma_f32_16x16x32_bf16 v[148:151], a[248:251], a[168:171], v[148:151]\n"
  "v_cvt_pk_bf16_f32 v26, v132, v133\n"
  "v_mfma_f32_16x16x32_bf16 v[148:151], a[252:255], a[172:175], v[148:151]\n"
  "v_exp_f32_e32 v134, v134\n"
  "v_mfma_f32_16x16x32_bf16 v[156:159], a[240:243], a[176:179], v[188:191]\n"
  "buffer_load_dwordx4 a[212:215], v225, s[40:43], s44 offen offset:1024 sc1\n"
  "v_mfma_f32_16x16x32_bf16 v[156:159], a[244:247], a[180:183], v[156:159]\n"
  "v_exp_f32_e32 v135, v135\n"
  "v_mfma_f32_16x16x32_bf16 v[156:159], a[248:251], a[184:187], v[156:159]\n"
  "v_cvt_pk_bf16_f32 v27, v134, v135\n"
  "s_waitcnt vmcnt(12)\n"
  "v_mfma_f32_16x16x32_bf16 v[156:159], a[252:255], a[188:191], v[156:159]\n"
  "v_exp_f32_e32 v140, v140\n"
  "v_mfma_f32_16x16x32_bf16 v[8:11], a[112:115], v[160:163], v[8:11]\n"
  "buffer_load_dwordx4 a[216:219], v225, s[40:43], s44 offen offset:2048 sc1\n"
  "v_mfma_f32_16x16x32_bf16 v[52:55], a[112:115], v[164:167], v[52:55]\n"
  "v_exp_f32_e32 v141, v141\n"
  "v_mfma_f32_16x16x32_bf16 v[12:15], a[116:119], v[160:163], v[12:15]\n"
  "v_cvt_pk_bf16_f32 v30, v140, v141\n"
  "v_mfma_f32_16x16x32_bf16 v[56:59], a[116:119], v[164:167], v[56:59]\n"
  "v_exp_f32_e32 v142, v142\n"
  "v_mfma_f32_16x16x32_bf16 v[16:19], a[120:123], v[160:163], v[16:19]\n"
  "buffer_load_dwordx4 a[220:223], v225, s[40:43], s44 offen offset:3072 sc1\n"
  "v_mfma_f32_16x16x32_bf16 v[60:63], a[120:123], v[164:167], v[60:63]\n"
  "v_exp_f32_e32 v143, v143\n"
  "v_mfma_f32_16x16x32_bf16 v[20:23], a[124:127], v[160:163], v[20:23]\n"
  "v_cvt_pk_bf16_f32 v31, v142, v143\n"
  "v_mfma_f32_16x16x32_bf16 v[196:199], a[124:127], v[164:167], v[196:199]\n"
  "v_exp_f32_e32 v148, v148\n"
  "v_mfma_f32_16x16x32_bf16 v[112:115], a[112:115], v[168:171], v[112:115]\n"
  "buffer_load_dwordx4 a[80:83], v227, s[40:43], s45 offen offset:0 sc1\n"
  "v_mfma_f32_16x16x32_bf16 v[80:83], v[172:175], a[112:115], v[80:83]\n"
  "v_exp_f32_e32 v149, v149\n"
  "v_mfma_f32_16x16x32_bf16 v[116:119], a[116:119], v[168:171], v[116:119]\n"
  "v_cvt_pk_bf16_f32 v34, v148, v149\n"
  "v_mfma_f32_16x16x32_bf16 v[84:87], v[172:175], a[116:119], v[84:87]\n"
  "v_exp_f32_e32 v150, v150\n"
  "v_mfma_f32_16x16x32_bf16 v[120:123], a[120:123], v[168:171], v[120:123]\n"
  "buffer_load_dwordx4 a[84:87], v227, s[40:43], s45 offen offset:1024 sc1\n"
  "v_mfma_f32_16x16x32_bf16 v[88:91], v[172:175], a[120:123], v[88:91]\n"
  "v_exp_f32_e32 v151, v151\n"
  "v_mfma_f32_16x16x32_bf16 v[124:127], a[124:127], v[168:171], v[124:127]\n"
  "v_cvt_pk_bf16_f32 v35, v150, v151\n"
  "s_waitcnt vmcnt(11)\n"
  "v_mfma_f32_16x16x32_bf16 v[92:95], v[172:175], a[124:127], v[92:95]\n"
  "v_exp_f32_e32 v156, v156\n"
  "v_smfmac_f32_16x16x64_bf16 v[200:203], v[208:211], v[168:175], v247\n"
  "buffer_load_dwordx4 a[88:91], v227, s[40:43], s45 offen offset:2048 sc1\n"
  "L_loop_%=:\n"
  "v_mfma_f32_16x16x32_bf16 v[128:131], a[192:195], a[128:131], v[176:179]\n"
  "v_exp_f32_e32 v157, v157\n"
  "v_mfma_f32_16x16x32_bf16 v[128:131], a[196:199], a[132:135], v[128:131]\n"
  "v_cvt_pk_bf16_f32 v38, v156, v157\n"
  "v_mfma_f32_16x16x32_bf16 v[128:131], a[200:203], a[136:139], v[128:131]\n"
  "v_exp_f32_e32 v158, v158\n"
  "v_mfma_f32_16x16x32_bf16 v[128:131], a[204:207], a[140:143], v[128:131]\n"
  "buffer_load_dwordx4 a[92:95], v227, s[40:43], s45 offen offset:3072 sc1\n"
  "v_mfma_f32_16x16x32_bf16 v[136:139], a[192:195], a[144:147], v[180:183]\n"
  "v_exp_f32_e32 v159, v159\n"
  "v_mfma_f32_16x16x32_bf16 v[136:139], a[196:199], a[148:151], v[136:139]\n"
  "v_cvt_pk_bf16_f32 v39, v158, v159\n"
  "s_xor_b32 s45, s48, s56\n"
  "v_mfma_f32_16x16x32_bf16 v[136:139], a[200:203], a[152:155], v[136:139]\n"
  "s_add_u32 s46, s46, 0x80\n"
  "v_mfma_f32_16x16x32_bf16 v[136:139], a[204:207], a[156:159], v[136:139]\n"
  "v_exp_f32_e32 v128, v128\n"
  "v_mfma_f32_16x16x32_bf16 v[144:147], a[192:195], a[160:163], v[184:187]\n"
  "buffer_load_dwordx4 a[224:227], v224, s[40:43], s45 offen offset:0 sc1\n"
  "v_mfma_f32_16x16x32_bf16 v[144:147], a[196:199], a[164:167], v[144:147]\n"
  "v_exp_f32_e32 v129, v129\n"
  "v_mfma_f32_16x16x32_bf16 v[144:147], a[200:203], a[168:171], v[144:147]\n"
  "v_cvt_pk_bf16_f32 v160, v128, v129\n"
  "v_mfma_f32_16x16x32_bf16 v[144:147], a[204:207], a[172:175], v[144:147]\n"
  "v_exp_f32_e32 v130, v130\n"
  "v_mfma_f32_16x16x32_bf16 v[152:155], a[192:195], a[176:179], v[188:191]\n"
  "buffer_load_dwordx4 a[228:231], v224, s[40:43], s45 offen offset:1024 sc1\n"
  "v_mfma_f32_16x16x32_bf16 v[152:155], a[196:199], a[180:183], v[152:155]\n"
  "v_exp_f32_e32 v131, v131\n"
  "v_mfma_f32_16x16x32_bf16 v[152:155], a[200:203], a[184:187], v[152:155]\n"
  "v_cvt_pk_bf16_f32 v161, v130, v131\n"
  "s_waitcnt vmcnt(11)\n"
  "v_mfma_f32_16x16x32_bf16 v[152:155], a[204:207], a[188:191], v[152:155]\n"
  "v_exp_f32_e32 v136, v136\n"
  "v_mfma_f32_16x16x32_bf16 v[228:231], a[64:67], v[24:27], v[228:231]\n"
  "buffer_load_dwordx4 a[232:235], v224, s[40:43], s45 offen offset:2048 sc1\n"
  "v_mfma_f32_16x16x32_bf16 v[4:7], a[64:67], v[28:31], v[4:7]\n"
  "v_exp_f32_e32 v137, v137\n"
  "v_mfma_f32_16x16x32_bf16 v[236:239], a[68:71], v[24:27], v[236:239]\n"
  "v_cvt_pk_bf16_f32 v164, v136, v137\n"
  "v_mfma_f32_16x16x32_bf16 v[40:43], a[68:71], v[28:31], v[40:43]\n"
  "v_exp_f32_e32 v138, v138\n"
  "v_mfma_f32_16x16x32_bf16 v[240:243], a[72:75], v[24:27], v[240:243]\n"
  "buffer_load_dwordx4 a[236:239], v224, s[40:43], s45 offen offset:3072 sc1\n"
  "v_mfma_f32_16x16x32_bf16 v[44:47], a[72:75], v[28:31], v[44:47]\n"
  "v_exp_f32_e32 v139, v139\n"
  "v_mfma_f32_16x16x32_bf16 v[204:207], a[76:79], v[24:27], v[204:207]\n"
  "v_cvt_pk_bf16_f32 v165, v138, v139\n"
  "v_mfma_f32_16x16x32_bf16 v[48:51], a[76:79], v[28:31], v[48:51]\n"
  "v_exp_f32_e32 v144, v144\n"
  "v_mfma_f32_16x16x32_bf16 v[96:99], a[64:67], v[32:35], v[96:99]\n"
  "buffer_load_dwordx4 a[96:99], v226, s[40:43], s44 offen offset:0 sc1\n"
  "v_mfma_f32_16x16x32_bf16 v[64:67], v[36:39], a[64:67], v[64:67]\n"
  "v_exp_f32_e32 v145, v145\n"
  "v_mfma_f32_16x16x32_bf16 v[100:103], a[68:71], v[32:35], v[100:103]\n"
  "v_cvt_pk_bf16_f32 v168, v144, v145\n"
  "v_mfma_f32_16x16x32_bf16 v[68:71], v[36:39], a[68:71], v[68:71]\n"
  "v_exp_f32_e32 v146, v146\n"
  "v_mfma_f32_16x16x32_bf16 v[104:107], a[72:75], v[32:35], v[104:107]\n"
  "buffer_load_dwordx4 a[100:103], v226, s[40:43], s44 offen offset:1024 sc1\n"
  "v_mfma_f32_16x16x32_bf16 v[72:75], v[36:39], a[72:75], v[72:75]\n"
  "v_exp_f32_e32 v147, v147\n"
  "v_mfma_f32_16x16x32_bf16 v[108:111], a[76:79], v[32:35], v[108:111]\n"
  "v_cvt_pk_bf16_f32 v169, v146, v147\n"
  "s_waitcnt vmcnt(10)\n"
  "v_mfma_f32_16x16x32_bf16 v[76:79], v[36:39], a[76:79], v[76:79]\n"
  "v_exp_f32_e32 v152, v152\n"
  "v_smfmac_f32_16x16x64_bf16 v[192:195], v[208:211], v[24:31], v247\n"
  "buffer_load_dwordx4 a[104:107], v226, s[40:43], s44 offen offset:2048 sc1\n"
  "v_mfma_f32_16x16x32_bf16 v[132:135], a[208:211], a[128:131], v[176:179]\n"
  "v_exp_f32_e32 v153, v153\n"
  "v_mfma_f32_16x16x32_bf16 v[132:135], a[212:215], a[132:135], v[132:135]\n"
  "v_cvt_pk_bf16_f32 v172, v152, v153\n"
  "s_add_u32 s48, s48, 0x4000\n"
  "v_mfma_f32_16x16x32_bf16 v[132:135], a[216:219], a[136:139], v[132:135]\n"
  "v_exp_f32_e32 v154, v154\n"
  "v_mfma_f32_16x16x32_bf16 v[132:135], a[220:223], a[140:143], v[132:135]\n"
  "buffer_load_dwordx4 a[108:111], v226, s[40:43], s44 offen offset:3072 sc1\n"
  "v_mfma_f32_16x16x32_bf16 v[140:143], a[208:211], a[144:147], v[180:183]\n"
  "v_exp_f32_e32 v155, v155\n"
  "v_mfma_f32_16x16x32_bf16 v[140:143], a[212:215], a[148:151], v[140:143]\n"
  "v_cvt_pk_bf16_f32 v173, v154, v155\n"
  "v_mfma_f32_16x16x32_bf16 v[140:143], a[216:219], a[152:155], v[140:143]\n"
  "v_cmp_ne_u64_e32 vcc, s[50:51], v[222:223]\n"
  "v_mfma_f32_16x16x32_bf16 v[140:143], a[220:223], a[156:159], v[140:143]\n"
  "v_exp_f32_e32 v132, v132\n"
  "v_mfma_f32_16x16x32_bf16 v[148:151], a[208:211], a[160:163], v[184:187]\n"
  "buffer_load_dwordx4 a[240:243], v225, s[40:43], s45 offen offset:0 sc1\n"
  "v_mfma_f32_16x16x32_bf16 v[148:151], a[212:215], a[164:167], v[148:151]\n"
  "v_exp_f32_e32 v133, v133\n"
  "v_mfma_f32_16x16x32_bf16 v[148:151], a[216:219], a[168:171], v[148:151]\n"
  "v_cvt_pk_bf16_f32 v162, v132, v133\n"
  "s_cbranch_vccnz L_spin_%=\n"
  "L_spinback_%=:\n"
  "v_mfma_f32_16x16x32_bf16 v[148:151], a[220:223], a[172:175], v[148:151]\n"
  "v_exp_f32_e32 v134, v134\n"
  "v_mfma_f32_16x16x32_bf16 v[156:159], a[208:211], a[176:179], v[188:191]\n"
  "buffer_load_dwordx4 a[244:247], v225, s[40:43], s45 offen offset:1024 sc1\n"
  "v_mfma_f32_16x16x32_bf16 v[156:159], a[212:215], a[180:183], v[156:159]\n"
  "v_exp_f32_e32 v135, v135\n"
  "v_mfma_f32_16x16x32_bf16 v[156:159], a[216:219], a[184:187], v[156:159]\n"
  "v_cvt_pk_bf16_f32 v163, v134, v135\n"
  "s_waitcnt vmcnt(10)\n"
  "v_mfma_f32_16x16x32_bf16 v[156:159], a[220:223], a[188:191], v[156:159]\n"
  "v_exp_f32_e32 v140, v140\n"
  "v_mfma_f32_16x16x32_bf16 v[8:11], a[80:83], v[24:27], v[8:11]\n"
  "buffer_load_dwordx4 a[248:251], v225, s[40:43], s45 offen offset:2048 sc1\n"
  "v_mfma_f32_16x16x32_bf16 v[52:55], a[80:83], v[28:31], v[52:55]\n"
  "v_exp_f32_e32 v141, v141\n"
  "v_mfma_f32_16x16x32_bf16 v[12:15], a[84:87], v[24:27], v[12:15]\n"
  "v_cvt_pk_bf16_f32 v166, v140, v141\n"
  "v_mfma_f32_16x16x32_bf16 v[56:59], a[84:87], v[28:31], v[56:59]\n"
  "v_exp_f32_e32 v142, v142\n"
  "v_mfma_f32_16x16x32_bf16 v[16:19], a[88:91], v[24:27], v[16:19]\n"
  "buffer_load_dwordx4 a[252:255], v225, s[40:43], s45 offen offset:3072 sc1\n"
  "v_mfma_f32_16x16x32_bf16 v[60:63], a[88:91], v[28:31], v[60:63]\n"
  "v_exp_f32_e32 v143, v143\n"
  "v_mfma_f32_16x16x32_bf16 v[20:23], a[92:95], v[24:27], v[20:23]\n"
  "v_cvt_pk_bf16_f32 v167, v142, v143\n"
  "v_mfma_f32_16x16x32_bf16 v[196:199], a[92:95], v[28:31], v[196:199]\n"
  "v_exp_f32_e32 v148, v148\n"
  "v_mfma_f32_16x16x32_bf16 v[112:115], a[80:83], v[32:35], v[112:115]\n"
  "buffer_load_dwordx4 a[112:115], v227, s[40:43], s44 offen offset:0 sc1\n"
  "v_mfma_f32_16x16x32_bf16 v[80:83], v[36:39], a[80:83], v[80:83]\n"
  "v_exp_f32_e32 v149, v149\n"
  "v_mfma_f32_16x16x32_bf16 v[116:119], a[84:87], v[32:35], v[116:119]\n"
  "v_cvt_pk_bf16_f32 v170, v148, v149\n"
  "v_mfma_f32_16x16x32_bf16 v[84:87], v[36:39], a[84:87], v[84:87]\n"
  "v_exp_f32_e32 v150, v150\n"
  "v_mfma_f32_16x16x32_bf16 v[120:123], a[88:91], v[32:35], v[120:123]\n"
  "buffer_load_dwordx4 a[116:119], v227, s[40:43], s44 offen offset:1024 sc1\n"
  "v_mfma_f32_16x16x32_bf16 v[88:91], v[36:39], a[88:91], v[88:91]\n"
  "v_exp_f32_e32 v151, v151\n"
  "v_mfma_f32_16x16x32_bf16 v[124:127], a[92:95], v[32:35], v[124:127]\n"
  "v_cvt_pk_bf16_f32 v171, v150, v151\n"
  "s_waitcnt vmcnt(10)\n"
  "v_mfma_f32_16x16x32_bf16 v[92:95], v[36:39], a[92:95], v[92:95]\n"
  "v_exp_f32_e32 v156, v156\n"
  "v_smfmac_f32_16x16x64_bf16 v[200:203], v[208:211], v[32:39], v247\n"
  "buffer_load_dwordx4 a[120:123], v227, s[40:43], s44 offen offset:2048 sc1\n"
  "v_mfma_f32_16x16x32_bf16 v[128:131], a[224:227], a[128:131], v[176:179]\n"
  "v_exp_f32_e32 v157, v157\n"
  "v_mfma_f32_16x16x32_bf16 v[128:131], a[228:231], a[132:135], v[128:131]\n"
  "v_cvt_pk_bf16_f32 v174, v156, v157\n"
  "v_mfma_f32_16x16x32_bf16 v[128:131], a[232:235], a[136:139], v[128:131]\n"
  "v_exp_f32_e32 v158, v158\n"
  "v_mfma_f32_16x16x32_bf16 v[128:131], a[236:239], a[140:143], v[128:131]\n"
  "buffer_load_dwordx4 a[124:127], v227, s[40:43], s44 offen offset:3072 sc1\n"
  "v_mfma_f32_16x16x32_bf16 v[136:139], a[224:227], a[144:147], v[180:183]\n"
  "v_exp_f32_e32 v159, v159\n"
  "v_mfma_f32_16x16x32_bf16 v[136:139], a[228:231], a[148:151], v[136:139]\n"
  "v_cvt_pk_bf16_f32 v175, v158, v159\n"
  "s_xor_b32 s44, s48, s56\n"
  "v_mfma_f32_16x16x32_bf16 v[136:139], a[232:235], a[152:155], v[136:139]\n"
  "s_and_b32 s57, s46, 0xf00\n"
  "v_mfma_f32_16x16x32_bf16 v[136:139], a[236:239], a[156:159], v[136:139]\n"
  "v_exp_f32_e32 v128, v128\n"
  "v_mfma_f32_16x16x32_bf16 v[144:147], a[224:227], a[160:163], v[184:187]\n"
  "buffer_load_dwordx4 a[192:195], v224, s[40:43], s44 offen offset:0 sc1\n"
  "v_mfma_f32_16x16x32_bf16 v[144:147], a[228:231], a[164:167], v[144:147]\n"
  "v_exp_f32_e32 v129, v129\n"
  "v_mfma_f32_16x16x32_bf16 v[144:147], a[232:235], a[168:171], v[144:147]\n"
  "v_cvt_pk_bf16_f32 v24, v128, v129\n"
  "v_mfma_f32_16x16x32_bf16 v[144:147], a[236:239], a[172:175], v[144:147]\n"
  "v_exp_f32_e32 v130, v130\n"
  "v_mfma_f32_16x16x32_bf16 v[152:155], a[224:227], a[176:179], v[188:191]\n"
  "buffer_load_dwordx4 a[196:199], v224, s[40:43], s44 offen offset:1024 sc1\n"
  "v_mfma_f32_16x16x32_bf16 v[152:155], a[228:231], a[180:183], v[152:155]\n"
  "v_exp_f32_e32 v131, v131\n"
  "v_mfma_f32_16x16x32_bf16 v[152:155], a[232:235], a[184:187], v[152:155]\n"
  "v_cvt_pk_bf16_f32 v25, v130, v131\n"
  "s_waitcnt vmcnt(10)\n"
  "v_mfma_f32_16x16x32_bf16 v[152:155], a[236:239], a[188:191], v[152:155]\n"
  "v_exp_f32_e32 v136, v136\n"
  "v_mfma_f32_16x16x32_bf16 v[228:231], a[96:99], v[160:163], v[228:231]\n"
  "buffer_load_dwordx4 a[200:203], v224, s[40:43], s44 offen offset:2048 sc1\n"
  "v_mfma_f32_16x16x32_bf16 v[4:7], a[96:99], v[164:167], v[4:7]\n"
  "v_exp_f32_e32 v137, v137\n"
  "v_mfma_f32_16x16x32_bf16 v[236:239], a[100:103], v[160:163], v[236:239]\n"
  "v_cvt_pk_bf16_f32 v28, v136, v137\n"
  "v_mfma_f32_16x16x32_bf16 v[40:43], a[100:103], v[164:167], v[40:43]\n"
  "v_exp_f32_e32 v138, v138\n"
  "v_mfma_f32_16x16x32_bf16 v[240:243], a[104:107], v[160:163], v[240:243]\n"
  "buffer_load_dwordx4 a[204:207], v224, s[40:43], s44 offen offset:3072 sc1\n"
  "v_mfma_f32_16x16x32_bf16 v[44:47], a[104:107], v[164:167], v[44:47]\n"
  "v_exp_f32_e32 v139, v139\n"
  "v_mfma_f32_16x16x32_bf16 v[204:207], a[108:111], v[160:163], v[204:207]\n"
  "v_cvt_pk_bf16_f32 v29, v138, v139\n"
  "v_mfma_f32_16x16x32_bf16 v[48:51], a[108:111], v[164:167], v[48:51]\n"
  "v_exp_f32_e32 v144, v144\n"
  "v_mfma_f32_16x16x32_bf16 v[96:99], a[96:99], v[168:171], v[96:99]\n"
  "buffer_load_dwordx4 a[64:67], v226, s[40:43], s45 offen offset:0 sc1\n"
  "v_mfma_f32_16x16x32_bf16 v[64:67], v[172:175], a[96:99], v[64:67]\n"
  "v_exp_f32_e32 v145, v145\n"
  "v_mfma_f32_16x16x32_bf16 v[100:103], a[100:103], v[168:171], v[100:103]\n"
  "v_cvt_pk_bf16_f32 v32, v144, v145\n"
  "v_mfma_f32_16x16x32_bf16 v[68:71], v[172:175], a[100:103], v[68:71]\n"
  "v_exp_f32_e32 v146, v146\n"
  "v_mfma_f32_16x16x32_bf16 v[104:107], a[104:107], v[168:171], v[104:107]\n"
  "buffer_load_dwordx4 a[68:71], v226, s[40:43], s45 offen offset:1024 sc1\n"
  "v_mfma_f32_16x16x32_bf16 v[72:75], v[172:175], a[104:107], v[72:75]\n"
  "v_exp_f32_e32 v147, v147\n"
  "v_mfma_f32_16x16x32_bf16 v[108:111], a[108:111], v[168:171], v[108:111]\n"
  "v_cvt_pk_bf16_f32 v33, v146, v147\n"
  "s_waitcnt vmcnt(10)\n"
  "v_mfma_f32_16x16x32_bf16 v[76:79], v[172:175], a[108:111], v[76:79]\n"
  "v_exp_f32_e32 v152, v152\n"
  "v_smfmac_f32_16x16x64_bf16 v[192:195], v[208:211], v[160:167], v247\n"
  "buffer_load_dwordx4 a[72:75], v226, s[40:43], s45 offen offset:2048 sc1\n"
  "v_mfma_f32_16x16x32_bf16 v[132:135], a[240:243], a[128:131], v[176:179]\n"
  "v_exp_f32_e32 v153, v153\n"
  "v_mfma_f32_16x16x32_bf16 v[132:135], a[244:247], a[132:135], v[132:135]\n"
  "v_cvt_pk_bf16_f32 v36, v152, v153\n"
  "s_add_u32 s48, s48, 0x4000\n"
  "v_mfma_f32_16x16x32_bf16 v[132:135], a[248:251], a[136:139], v[132:135]\n"
  "v_exp_f32_e32 v154, v154\n"
  "v_mfma_f32_16x16x32_bf16 v[132:135], a[252:255], a[140:143], v[132:135]\n"
  "buffer_load_dwordx4 a[76:79], v226, s[40:43], s45 offen offset:3072 sc1\n"
  "v_mfma_f32_16x16x32_bf16 v[140:143], a[240:243], a[144:147], v[180:183]\n"
  "v_exp_f32_e32 v155, v155\n"
  "v_mfma_f32_16x16x32_bf16 v[140:143], a[244:247], a[148:151], v[140:143]\n"
  "v_cvt_pk_bf16_f32 v37, v154, v155\n"
  "v_mfma_f32_16x16x32_bf16 v[140:143], a[248:251], a[152:155], v[140:143]\n"
  "buffer_load_dwordx2 v[222:223], v235, s[52:55], s57 offen sc1\n"
  "v_mfma_f32_16x16x32_bf16 v[140:143], a[252:255], a[156:159], v[140:143]\n"
  "v_exp_f32_e32 v132, v132\n"
  "v_mfma_f32_16x16x32_bf16 v[148:151], a[240:243], a[160:163], v[184:187]\n"
  "buffer_load_dwordx4 a[208:211], v225, s[40:43], s44 offen offset:0 sc1\n"
  "v_mfma_f32_16x16x32_bf16 v[148:151], a[244:247], a[164:167], v[148:151]\n"
  "v_exp_f32_e32 v133, v133\n"
  "v_mfma_f32_16x16x32_bf16 v[148:151], a[248:251], a[168:171], v[148:151]\n"
  "v_cvt_pk_bf16_f32 v26, v132, v133\n"
  "v_mfma_f32_16x16x32_bf16 v[148:151], a[252:255], a[172:175], v[148:151]\n"
  "v_exp_f32_e32 v134, v134\n"
  "v_mfma_f32_16x16x32_bf16 v[156:159], a[240:243], a[176:179], v[188:191]\n"
  "buffer_load_dwordx4 a[212:215], v225, s[40:43], s44 offen offset:1024 sc1\n"
  "v_mfma_f32_16x16x32_bf16 v[156:159], a[244:247], a[180:183], v[156:159]\n"
  "v_exp_f32_e32 v135, v135\n"
  "v_mfma_f32_16x16x32_bf16 v[156:159], a[248:251], a[184:187], v[156:159]\n"
  "v_cvt_pk_bf16_f32 v27, v134, v135\n"
  "s_waitcnt vmcnt(11)\n"
  "v_mfma_f32_16x16x32_bf16 v[156:159], a[252:255], a[188:191], v[156:159]\n"
  "v_exp_f32_e32 v140, v140\n"
  "v_mfma_f32_16x16x32_bf16 v[8:11], a[112:115], v[160:163], v[8:11]\n"
  "buffer_load_dwordx4 a[216:219], v225, s[40:43], s44 offen offset:2048 sc1\n"
  "v_mfma_f32_16x16x32_bf16 v[52:55], a[112:115], v[164:167], v[52:55]\n"
  "v_exp_f32_e32 v141, v141\n"
  "v_mfma_f32_16x16x32_bf16 v[12:15], a[116:119], v[160:163], v[12:15]\n"
  "v_cvt_pk_bf16_f32 v30, v140, v141\n"
  "v_mfma_f32_16x16x32_bf16 v[56:59], a[116:119], v[164:167], v[56:59]\n"
  "v_exp_f32_e32 v142, v142\n"
  "v_mfma_f32_16x16x32_bf16 v[16:19], a[120:123], v[160:163], v[16:19]\n"
  "buffer_load_dwordx4 a[220:223], v225, s[40:43], s44 offen offset:3072 sc1\n"
  "v_mfma_f32_16x16x32_bf16 v[60:63], a[120:123], v[164:167], v[60:63]\n"
  "v_exp_f32_e32 v143, v143\n"
  "v_mfma_f32_16x16x32_bf16 v[20:23], a[124:127], v[160:163], v[20:23]\n"
  "v_cvt_pk_bf16_f32 v31, v142, v143\n"
  "v_mfma_f32_16x16x32_bf16 v[196:199], a[124:127], v[164:167], v[196:199]\n"
  "v_exp_f32_e32 v148, v148\n"
  "v_mfma_f32_16x16x32_bf16 v[112:115], a[112:115], v[168:171], v[112:115]\n"
  "buffer_load_dwordx4 a[80:83], v227, s[40:43], s45 offen offset:0 sc1\n"
  "v_mfma_f32_16x16x32_bf16 v[80:83], v[172:175], a[112:115], v[80:83]\n"
  "v_exp_f32_e32 v149, v149\n"
  "v_mfma_f32_16x16x32_bf16 v[116:119], a[116:119], v[168:171], v[116:119]\n"
  "v_cvt_pk_bf16_f32 v34, v148, v149\n"
  "v_mfma_f32_16x16x32_bf16 v[84:87], v[172:175], a[116:119], v[84:87]\n"
  "v_exp_f32_e32 v150, v150\n"
  "v_mfma_f32_16x16x32_bf16 v[120:123], a[120:123], v[168:171], v[120:123]\n"
  "buffer_load_dwordx4 a[84:87], v227, s[40:43], s45 offen offset:1024 sc1\n"
  "v_mfma_f32_16x16x32_bf16 v[88:91], v[172:175], a[120:123], v[88:91]\n"
  "v_exp_f32_e32 v151, v151\n"
  "v_mfma_f32_16x16x32_bf16 v[124:127], a[124:127], v[168:171], v[124:127]\n"
  "v_cvt_pk_bf16_f32 v35, v150, v151\n"
  "s_waitcnt vmcnt(11)\n"
  "v_mfma_f32_16x16x32_bf16 v[92:95], v[172:175], a[124:127], v[92:95]\n"
  "v_exp_f32_e32 v156, v156\n"
  "v_smfmac_f32_16x16x64_bf16 v[200:203], v[208:211], v[168:175], v247\n"
  "buffer_load_dwordx4 a[88:91], v227, s[40:43], s45 offen offset:2048 sc1\n"
  "s_cmp_lg_u32 s46, 0x400\n"
  "s_cbranch_scc1 L_loop_%=\n"
  "L_tail_%=:\n"
  "v_mfma_f32_16x16x32_bf16 v[128:131], a[192:195], a[128:131], v[176:179]\n"
  "v_exp_f32_e32 v157, v157\n"
  "v_mfma_f32_16x16x32_bf16 v[128:131], a[196:199], a[132:135], v[128:131]\n"
  "v_cvt_pk_bf16_f32 v38, v156, v157\n"
  "v_mfma_f32_16x16x32_bf16 v[128:131], a[200:203], a[136:139], v[128:131]\n"
  "v_exp_f32_e32 v158, v158\n"
  "v_mfma_f32_16x16x32_bf16 v[128:131], a[204:207], a[140:143], v[128:131]\n"
  "buffer_load_dwordx4 a[92:95], v227, s[40:43], s45 offen offset:3072 sc1\n"
  "v_mfma_f32_16x16x32_bf16 v[136:139], a[192:195], a[144:147], v[180:183]\n"
  "v_exp_f32_e32 v159, v159\n"
  "v_mfma_f32_16x16x32_bf16 v[136:139], a[196:199], a[148:151], v[136:139]\n"
  "v_cvt_pk_bf16_f32 v39, v158, v159\n"
  "s_xor_b32 s45, s48, s56\n"
  "v_mfma_f32_16x16x32_bf16 v[136:139], a[200:203], a[152:155], v[136:139]\n"
  "v_mfma_f32_16x16x32_bf16 v[136:139], a[204:207], a[156:159], v[136:139]\n"
  "v_exp_f32_e32 v128, v128\n"
  "v_mfma_f32_16x16x32_bf16 v[144:147], a[192:195], a[160:163], v[184:187]\n"
  "buffer_load_dwordx4 a[224:227], v224, s[40:43], s45 offen offset:0 sc1\n"
  "v_mfma_f32_16x16x32_bf16 v[144:147], a[196:199], a[164:167], v[144:147]\n"
  "v_exp_f32_e32 v129, v129\n"
  "v_mfma_f32_16x16x32_bf16 v[144:147], a[200:203], a[168:171], v[144:147]\n"
  "v_cvt_pk_bf16_f32 v160, v128, v129\n"
  "v_mfma_f32_16x16x32_bf16 v[144:147], a[204:207], a[172:175], v[144:147]\n"
  "v_exp_f32_e32 v130, v130\n"
  "v_mfma_f32_16x16x32_bf16 v[152:155], a[192:195], a[176:179], v[188:191]\n"
  "buffer_load_dwordx4 a[228:231], v224, s[40:43], s45 offen offset:1024 sc1\n"
  "v_mfma_f32_16x16x32_bf16 v[152:155], a[196:199], a[180:183], v[152:155]\n"
  "v_exp_f32_e32 v131, v131\n"
  "v_mfma_f32_16x16x32_bf16 v[152:155], a[200:203], a[184:187], v[152:155]\n"
  "v_cvt_pk_bf16_f32 v161, v130, v131\n"
  "s_waitcnt vmcnt(11)\n"
  "v_mfma_f32_16x16x32_bf16 v[152:155], a[204:207], a[188:191], v[152:155]\n"
  "v_exp_f32_e32 v136, v136\n"
  "v_mfma_f32_16x16x32_bf16 v[228:231], a[64:67], v[24:27], v[228:231]\n"
  "buffer_load_dwordx4 a[232:235], v224, s[40:43], s45 offen offset:2048 sc1\n"
  "v_mfma_f32_16x16x32_bf16 v[4:7], a[64:67], v[28:31], v[4:7]\n"
  "v_exp_f32_e32 v137, v137\n"
  "v_mfma_f32_16x16x32_bf16 v[236:239], a[68:71], v[24:27], v[236:239]\n"
  "v_cvt_pk_bf16_f32 v164, v136, v137\n"
  "v_mfma_f32_16x16x32_bf16 v[40:43], a[68:71], v[28:31], v[40:43]\n"
  "v_exp_f32_e32 v138, v138\n"
  "v_mfma_f32_16x16x32_bf16 v[240:243], a[72:75], v[24:27], v[240:243]\n"
  "buffer_load_dwordx4 a[236:239], v224, s[40:43], s45 offen offset:3072 sc1\n"
  "v_mfma_f32_16x16x32_bf16 v[44:47], a[72:75], v[28:31], v[44:47]\n"
  "v_exp_f32_e32 v139, v139\n"
  "v_mfma_f32_16x16x32_bf16 v[204:207], a[76:79], v[24:27], v[204:207]\n"
  "v_cvt_pk_bf16_f32 v165, v138, v139\n"
  "v_mfma_f32_16x16x32_bf16 v[48:51], a[76:79], v[28:31], v[48:51]\n"
  "v_exp_f32_e32 v144, v144\n"
  "v_mfma_f32_16x16x32_bf16 v[96:99], a[64:67], v[32:35], v[96:99]\n"
  "buffer_load_dwordx4 a[240:243], v225, s[40:43], s45 offen offset:0 sc1\n"
  "v_mfma_f32_16x16x32_bf16 v[64:67], v[36:39], a[64:67], v[64:67]\n"
  "v_exp_f32_e32 v145, v145\n"
  "v_mfma_f32_16x16x32_bf16 v[100:103], a[68:71], v[32:35], v[100:103]\n"
  "v_cvt_pk_bf16_f32 v168, v144, v145\n"
  "v_mfma_f32_16x16x32_bf16 v[68:71], v[36:39], a[68:71], v[68:71]\n"
  "v_exp_f32_e32 v146, v146\n"
  "v_mfma_f32_16x16x32_bf16 v[104:107], a[72:75], v[32:35], v[104:107]\n"
  "buffer_load_dwordx4 a[244:247], v225, s[40:43], s45 offen offset:1024 sc1\n"
  "v_mfma_f32_16x16x32_bf16 v[72:75], v[36:39], a[72:75], v[72:75]\n"
  "v_exp_f32_e32 v147, v147\n"
  "v_mfma_f32_16x16x32_bf16 v[108:111], a[76:79], v[32:35], v[108:111]\n"
  "v_cvt_pk_bf16_f32 v169, v146, v147\n"
  "s_waitcnt vmcnt(10)\n"
  "v_mfma_f32_16x16x32_bf16 v[76:79], v[36:39], a[76:79], v[76:79]\n"
  "v_exp_f32_e32 v152, v152\n"
  "v_smfmac_f32_16x16x64_bf16 v[192:195], v[208:211], v[24:31], v247\n"
  "buffer_load_dwordx4 a[248:251], v225, s[40:43], s45 offen offset:2048 sc1\n"
  "v_mfma_f32_16x16x32_bf16 v[132:135], a[208:211], a[128:131], v[176:179]\n"
  "v_exp_f32_e32 v153, v153\n"
  "v_mfma_f32_16x16x32_bf16 v[132:135], a[212:215], a[132:135], v[132:135]\n"
  "v_cvt_pk_bf16_f32 v172, v152, v153\n"
  "v_mfma_f32_16x16x32_bf16 v[132:135], a[216:219], a[136:139], v[132:135]\n"
  "v_exp_f32_e32 v154, v154\n"
  "v_mfma_f32_16x16x32_bf16 v[132:135], a[220:223], a[140:143], v[132:135]\n"
  "buffer_load_dwordx4 a[252:255], v225, s[40:43], s45 offen offset:3072 sc1\n"
  "v_mfma_f32_16x16x32_bf16 v[140:143], a[208:211], a[144:147], v[180:183]\n"
  "v_exp_f32_e32 v155, v155\n"
  "v_mfma_f32_16x16x32_bf16 v[140:143], a[212:215], a[148:151], v[140:143]\n"
  "v_cvt_pk_bf16_f32 v173, v154, v155\n"
  "v_mfma_f32_16x16x32_bf16 v[140:143], a[216:219], a[152:155], v[140:143]\n"
  "v_mfma_f32_16x16x32_bf16 v[140:143], a[220:223], a[156:159], v[140:143]\n"
  "s_nop 0\n"
  "v_exp_f32_e32 v132, v132\n"
  "v_mfma_f32_16x16x32_bf16 v[148:151], a[208:211], a[160:163], v[184:187]\n"
  "buffer_load_dwordx4 a[96:99], v226, s[40:43], s44 offen offset:0 sc1\n"
  "v_mfma_f32_16x16x32_bf16 v[148:151], a[212:215], a[164:167], v[148:151]\n"
  "v_exp_f32_e32 v133, v133\n"
  "v_mfma_f32_16x16x32_bf16 v[148:151], a[216:219], a[168:171], v[148:151]\n"
  "v_cvt_pk_bf16_f32 v162, v132, v133\n"
  "s_mul_i32 m0, s49, 12544\n"
  "v_mfma_f32_16x16x32_bf16 v[148:151], a[220:223], a[172:175], v[148:151]\n"
  "v_exp_f32_e32 v134, v134\n"
  "v_mfma_f32_16x16x32_bf16 v[156:159], a[208:211], a[176:179], v[188:191]\n"
  "buffer_load_dwordx4 a[64:67], v226, s[40:43], s45 offen offset:0 sc1\n"
  "v_mfma_f32_16x16x32_bf16 v[156:159], a[212:215], a[180:183], v[156:159]\n"
  "v_exp_f32_e32 v135, v135\n"
  "v_mfma_f32_16x16x32_bf16 v[156:159], a[216:219], a[184:187], v[156:159]\n"
  "v_cvt_pk_bf16_f32 v163, v134, v135\n"
  "s_waitcnt vmcnt(10)\n"
  "v_mfma_f32_16x16x32_bf16 v[156:159], a[220:223], a[188:191], v[156:159]\n"
  "v_exp_f32_e32 v140, v140\n"
  "v_mfma_f32_16x16x32_bf16 v[8:11], a[80:83], v[24:27], v[8:11]\n"
  "buffer_load_dwordx4 a[100:103], v226, s[40:43], s44 offen offset:1024 sc1\n"
  "v_mfma_f32_16x16x32_bf16 v[52:55], a[80:83], v[28:31], v[52:55]\n"
  "v_exp_f32_e32 v141, v141\n"
  "v_mfma_f32_16x16x32_bf16 v[12:15], a[84:87], v[24:27], v[12:15]\n"
  "v_cvt_pk_bf16_f32 v166, v140, v141\n"
  "v_mfma_f32_16x16x32_bf16 v[56:59], a[84:87], v[28:31], v[56:59]\n"
  "v_exp_f32_e32 v142, v142\n"
  "v_mfma_f32_16x16x32_bf16 v[16:19], a[88:91], v[24:27], v[16:19]\n"
  "buffer_load_dwordx4 a[68:71], v226, s[40:43], s45 offen offset:1024 sc1\n"
  "v_mfma_f32_16x16x32_bf16 v[60:63], a[88:91], v[28:31], v[60:63]\n"
  "v_exp_f32_e32 v143, v143\n"
  "v_mfma_f32_16x16x32_bf16 v[20:23], a[92:95], v[24:27], v[20:23]\n"
  "v_cvt_pk_bf16_f32 v167, v142, v143\n"
  "v_mfma_f32_16x16x32_bf16 v[196:199], a[92:95], v[28:31], v[196:199]\n"
  "v_exp_f32_e32 v148, v148\n"
  "v_mfma_f32_16x16x32_bf16 v[112:115], a[80:83], v[32:35], v[112:115]\n"
  "buffer_load_dwordx4 a[104:107], v226, s[40:43], s44 offen offset:2048 sc1\n"
  "v_mfma_f32_16x16x32_bf16 v[80:83], v[36:39], a[80:83], v[80:83]\n"
  "v_exp_f32_e32 v149, v149\n"
  "v_mfma_f32_16x16x32_bf16 v[116:119], a[84:87], v[32:35], v[116:119]\n"
  "v_cvt_pk_bf16_f32 v170, v148, v149\n"
  "v_mfma_f32_16x16x32_bf16 v[84:87], v[36:39], a[84:87], v[84:87]\n"
  "v_exp_f32_e32 v150, v150\n"
  "v_mfma_f32_16x16x32_bf16 v[120:123], a[88:91], v[32:35], v[120:123]\n"
  "buffer_load_dwordx4 a[72:75], v226, s[40:43], s45 offen offset:2048 sc1\n"
  "v_mfma_f32_16x16x32_bf16 v[88:91], v[36:39], a[88:91], v[88:91]\n"
  "v_exp_f32_e32 v151, v151\n"
  "v_mfma_f32_16x16x32_bf16 v[124:127], a[92:95], v[32:35], v[124:127]\n"
  "v_cvt_pk_bf16_f32 v171, v150, v151\n"
  "s_waitcnt vmcnt(10)\n"
  "v_mfma_f32_16x16x32_bf16 v[92:95], v[36:39], a[92:95], v[92:95]\n"
  "v_exp_f32_e32 v156, v156\n"
  "v_smfmac_f32_16x16x64_bf16 v[200:203], v[208:211], v[32:39], v247\n"
  "buffer_load_dwordx4 a[108:111], v226, s[40:43], s44 offen offset:3072 sc1\n"
  "v_mfma_f32_16x16x32_bf16 v[128:131], a[224:227], a[128:131], v[176:179]\n"
  "v_exp_f32_e32 v157, v157\n"
  "v_mfma_f32_16x16x32_bf16 v[128:131], a[228:231], a[132:135], v[128:131]\n"
  "v_exp_f32_e32 v158, v158\n"
  "v_mfma_f32_16x16x32_bf16 v[128:131], a[232:235], a[136:139], v[128:131]\n"
  "v_cvt_pk_bf16_f32 v174, v156, v157\n"
  "v_mfma_f32_16x16x32_bf16 v[128:131], a[236:239], a[140:143], v[128:131]\n"
  "buffer_load_dwordx4 a[76:79], v226, s[40:43], s45 offen offset:3072 sc1\n"
  "v_mfma_f32_16x16x32_bf16 v[136:139], a[224:227], a[144:147], v[180:183]\n"
  "v_exp_f32_e32 v159, v159\n"
  "v_mfma_f32_16x16x32_bf16 v[136:139], a[228:231], a[148:151], v[136:139]\n"
  "v_cvt_pk_bf16_f32 v175, v158, v159\n"
  "v_mfma_f32_16x16x32_bf16 v[136:139], a[232:235], a[152:155], v[136:139]\n"
  "s_nop 1\n"
  "v_exp_f32_e32 v128, v128\n"
  "v_mfma_f32_16x16x32_bf16 v[136:139], a[236:239], a[156:159], v[136:139]\n"
  "v_exp_f32_e32 v129, v129\n"
  "v_mfma_f32_16x16x32_bf16 v[144:147], a[224:227], a[160:163], v[184:187]\n"
  "buffer_load_dwordx4 a[112:115], v227, s[40:43], s44 offen offset:0 sc1\n"
  "v_mfma_f32_16x16x32_bf16 v[144:147], a[228:231], a[164:167], v[144:147]\n"
  "v_exp_f32_e32 v130, v130\n"
  "v_mfma_f32_16x16x32_bf16 v[144:147], a[232:235], a[168:171], v[144:147]\n"
  "v_cvt_pk_bf16_f32 v24, v128, v129\n"
  "v_mfma_f32_16x16x32_bf16 v[144:147], a[236:239], a[172:175], v[144:147]\n"
  "v_exp_f32_e32 v131, v131\n"
  "s_waitcnt vmcnt(11)\n"
  "v_mfma_f32_16x16x32_bf16 v[132:135], a[240:243], a[128:131], v[176:179]\n"
  "buffer_load_dwordx4 a[80:83], v227, s[40:43], s45 offen offset:0 sc1\n"
  "v_cvt_pk_bf16_f32 v25, v130, v131\n"
  "v_mfma_f32_16x16x32_bf16 v[132:135], a[244:247], a[132:135], v[132:135]\n"
  "v_exp_f32_e32 v136, v136\n"
  "s_waitcnt vmcnt(10)\n"
  "v_mfma_f32_16x16x32_bf16 v[132:135], a[248:251], a[136:139], v[132:135]\n"
  "v_exp_f32_e32 v137, v137\n"
  "v_mfma_f32_16x16x32_bf16 v[132:135], a[252:255], a[140:143], v[132:135]\n"
  "v_exp_f32_e32 v138, v138\n"
  "v_mfma_f32_16x16x32_bf16 v[140:143], a[240:243], a[144:147], v[180:183]\n"
  "buffer_load_dwordx4 a[116:119], v227, s[40:43], s44 offen offset:1024 sc1\n"
  "v_cvt_pk_bf16_f32 v28, v136, v137\n"
  "v_mfma_f32_16x16x32_bf16 v[140:143], a[244:247], a[148:151], v[140:143]\n"
  "v_exp_f32_e32 v139, v139\n"
  "v_mfma_f32_16x16x32_bf16 v[140:143], a[248:251], a[152:155], v[140:143]\n"
  "s_nop 0\n"
  "v_exp_f32_e32 v132, v132\n"
  "v_mfma_f32_16x16x32_bf16 v[140:143], a[252:255], a[156:159], v[140:143]\n"
  "v_exp_f32_e32 v133, v133\n"
  "v_smfmac_f32_16x16x64_bf16 v[192:195], v[208:211], v[160:167], v247\n"
  "buffer_load_dwordx4 a[84:87], v227, s[40:43], s45 offen offset:1024 sc1\n"
  "v_cvt_pk_bf16_f32 v29, v138, v139\n"
  "s_waitcnt vmcnt(11)\n"
  "v_mfma_f32_16x16x32_bf16 v[228:231], a[96:99], v[160:163], v[228:231]\n"
  "v_exp_f32_e32 v134, v134\n"
  "v_mfma_f32_16x16x32_bf16 v[4:7], a[96:99], v[164:167], v[4:7]\n"
  "v_cvt_pk_bf16_f32 v26, v132, v133\n"
  "v_exp_f32_e32 v144, v144\n"
  "v_mfma_f32_16x16x32_bf16 v[96:99], a[96:99], v[168:171], v[96:99]\n"
  "v_exp_f32_e32 v135, v135\n"
  "v_mfma_f32_16x16x32_bf16 v[148:151], a[240:243], a[160:163], v[184:187]\n"
  "buffer_load_dwordx4 a[120:123], v227, s[40:43], s44 offen offset:2048 sc1\n"
  "v_cvt_pk_bf16_f32 v27, v134, v135\n"
  "v_mfma_f32_16x16x32_bf16 v[148:151], a[244:247], a[164:167], v[148:151]\n"
  "v_exp_f32_e32 v140, v140\n"
  "v_mfma_f32_16x16x32_bf16 v[148:151], a[248:251], a[168:171], v[148:151]\n"
  "v_exp_f32_e32 v141, v141\n"
  "v_mfma_f32_16x16x32_bf16 v[148:151], a[252:255], a[172:175], v[148:151]\n"
  "v_exp_f32_e32 v142, v142\n"
  "v_smfmac_f32_16x16x64_bf16 v[200:203], v[208:211], v[168:175], v247\n"
  "buffer_load_dwordx4 a[88:91], v227, s[40:43], s45 offen offset:2048 sc1\n"
  "v_cvt_pk_bf16_f32 v30, v140, v141\n"
  "s_waitcnt vmcnt(11)\n"
  "v_mfma_f32_16x16x32_bf16 v[236:239], a[100:103], v[160:163], v[236:239]\n"
  "v_exp_f32_e32 v143, v143\n"
  "v_mfma_f32_16x16x32_bf16 v[40:43], a[100:103], v[164:167], v[40:43]\n"
  "v_cvt_pk_bf16_f32 v31, v142, v143\n"
  "v_exp_f32_e32 v145, v145\n"
  "v_mfma_f32_16x16x32_bf16 v[100:103], a[100:103], v[168:171], v[100:103]\n"
  "v_exp_f32_e32 v146, v146\n"
  "v_cvt_pk_bf16_f32 v32, v144, v145\n"
  "v_mfma_f32_16x16x32_bf16 v[228:231], a[64:67], v[24:27], v[228:231]\n"
  "buffer_load_dwordx4 a[124:127], v227, s[40:43], s44 offen offset:3072 sc1\n"
  "v_exp_f32_e32 v147, v147\n"
  "v_mfma_f32_16x16x32_bf16 v[4:7], a[64:67], v[28:31], v[4:7]\n"
  "v_cvt_pk_bf16_f32 v33, v146, v147\n"
  "v_exp_f32_e32 v148, v148\n"
  "v_mfma_f32_16x16x32_bf16 v[152:155], a[224:227], a[176:179], v[188:191]\n"
  "v_exp_f32_e32 v149, v149\n"
  "v_mfma_f32_16x16x32_bf16 v[152:155], a[228:231], a[180:183], v[152:155]\n"
  "v_exp_f32_e32 v150, v150\n"
  "v_cvt_pk_bf16_f32 v34, v148, v149\n"
  "v_mfma_f32_16x16x32_bf16 v[152:155], a[232:235], a[184:187], v[152:155]\n"
  "buffer_load_dwordx4 a[92:95], v227, s[40:43], s45 offen offset:3072 sc1\n"
  "ds_write_addtid_b32 v228 offset:0\n"
  "ds_write_addtid_b32 v229 offset:6176\n"
  "v_mfma_f32_16x16x32_bf16 v[152:155], a[236:239], a[188:191], v[152:155]\n"
  "ds_write_addtid_b32 v230 offset:50304\n"
  "ds_write_addtid_b32 v231 offset:56480\n"
  "v_smfmac_f32_16x16x64_bf16 v[192:195], v[208:211], v[24:31], v247\n"
  "ds_write_addtid_b32 v4 offset:2048\n"
  "v_exp_f32_e32 v151, v151\n"
  "s_waitcnt vmcnt(12)\n"
  "v_mfma_f32_16x16x32_bf16 v[236:239], a[68:71], v[24:27], v[236:239]\n"
  "ds_write_addtid_b32 v5 offset:8224\n"
  "ds_write_addtid_b32 v6 offset:52352\n"
  "v_cvt_pk_bf16_f32 v35, v150, v151\n"
  "v_mfma_f32_16x16x32_bf16 v[40:43], a[68:71], v[28:31], v[40:43]\n"
  "ds_write_addtid_b32 v7 offset:58528\n"
  "s_waitcnt vmcnt(11)\n"
  "v_mfma_f32_16x16x32_bf16 v[240:243], a[104:107], v[160:163], v[240:243]\n"
  "v_exp_f32_e32 v152, v152\n"
  "v_mfma_f32_16x16x32_bf16 v[44:47], a[104:107], v[164:167], v[44:47]\n"
  "v_add_f32_e32 v176, v192, v193\n"
  "v_mov_b32_e32 v177, v212\n"
  "ds_write_b64 v232, v[176:177]\n"
  "v_mfma_f32_16x16x32_bf16 v[104:107], a[104:107], v[168:171], v[104:107]\n"
  "ds_write_addtid_b32 v236 offset:256\n"
  "ds_write_addtid_b32 v237 offset:6432\n"
  "v_add_f32_e32 v178, v194, v195\n"
  "v_mov_b32_e32 v179, v213\n"
  "ds_write_b64 v233, v[178:179]\n"
  "v_mfma_f32_16x16x32_bf16 v[96:99], a[64:67], v[32:35], v[96:99]\n"
  "ds_write_addtid_b32 v238 offset:50560\n"
  "ds_write_addtid_b32 v239 offset:56736\n"
  "v_add_f32_e32 v216, v176, v178\n"
  "v_mfma_f32_16x16x32_bf16 v[100:103], a[68:71], v[32:35], v[100:103]\n"
  "ds_write_addtid_b32 v40 offset:2304\n"
  "v_mfma_f32_16x16x32_bf16 v[156:159], a[240:243], a[176:179], v[188:191]\n"
  "ds_write_addtid_b32 v41 offset:8480\n"
  "ds_write_addtid_b32 v42 offset:52608\n"
  "v_mfma_f32_16x16x32_bf16 v[156:159], a[244:247], a[180:183], v[156:159]\n"
  "ds_write_addtid_b32 v43 offset:58784\n"
  "v_mfma_f32_16x16x32_bf16 v[156:159], a[248:251], a[184:187], v[156:159]\n"
  "ds_write_addtid_b32 v96 offset:4096\n"
  "ds_write_addtid_b32 v97 offset:10272\n"
  "v_mfma_f32_16x16x32_bf16 v[156:159], a[252:255], a[188:191], v[156:159]\n"
  "ds_write_addtid_b32 v98 offset:54400\n"
  "ds_write_addtid_b32 v99 offset:60576\n"
  "s_waitcnt vmcnt(9)\n"
  "v_mfma_f32_16x16x32_bf16 v[204:207], a[108:111], v[160:163], v[204:207]\n"
  "ds_write_addtid_b32 v100 offset:4352\n"
  "v_mfma_f32_16x16x32_bf16 v[48:51], a[108:111], v[164:167], v[48:51]\n"
  "ds_write_addtid_b32 v101 offset:10528\n"
  "ds_write_addtid_b32 v102 offset:54656\n"
  "v_mfma_f32_16x16x32_bf16 v[108:111], a[108:111], v[168:171], v[108:111]\n"
  "ds_write_addtid_b32 v103 offset:60832\n"
  "v_exp_f32_e32 v156, v156\n"
  "v_mfma_f32_16x16x32_bf16 v[240:243], a[72:75], v[24:27], v[240:243]\n"
  "v_exp_f32_e32 v153, v153\n"
  "v_exp_f32_e32 v157, v157\n"
  "v_mfma_f32_16x16x32_bf16 v[44:47], a[72:75], v[28:31], v[44:47]\n"
  "v_exp_f32_e32 v154, v154\n"
  "v_exp_f32_e32 v158, v158\n"
  "v_mfma_f32_16x16x32_bf16 v[104:107], a[72:75], v[32:35], v[104:107]\n"
  "v_cvt_pk_bf16_f32 v36, v152, v153\n"
  "v_cvt_pk_bf16_f32 v38, v156, v157\n"
  "v_exp_f32_e32 v159, v159\n"
  "s_waitcnt vmcnt(7)\n"
  "v_mfma_f32_16x16x32_bf16 v[8:11], a[112:115], v[160:163], v[8:11]\n"
  "v_exp_f32_e32 v155, v155\n"
  "v_cvt_pk_bf16_f32 v39, v158, v159\n"
  "v_mfma_f32_16x16x32_bf16 v[52:55], a[112:115], v[164:167], v[52:55]\n"
  "ds_write_addtid_b32 v240 offset:512\n"
  "ds_write_addtid_b32 v241 offset:6688\n"
  "v_mfma_f32_16x16x32_bf16 v[112:115], a[112:115], v[168:171], v[112:115]\n"
  "ds_write_addtid_b32 v242 offset:50816\n"
  "ds_write_addtid_b32 v243 offset:56992\n"
  "v_mfma_f32_16x16x32_bf16 v[204:207], a[76:79], v[24:27], v[204:207]\n"
  "ds_write_addtid_b32 v44 offset:2560\n"
  "v_cvt_pk_bf16_f32 v37, v154, v155\n"
  "v_mfma_f32_16x16x32_bf16 v[48:51], a[76:79], v[28:31], v[48:51]\n"
  "ds_write_addtid_b32 v45 offset:8736\n"
  "ds_write_addtid_b32 v46 offset:52864\n"
  "v_mfma_f32_16x16x32_bf16 v[108:111], a[76:79], v[32:35], v[108:111]\n"
  "ds_write_addtid_b32 v47 offset:59040\n"
  "v_smfmac_f32_16x16x64_bf16 v[200:203], v[208:211], v[32:39], v247\n"
  "ds_write_addtid_b32 v104 offset:4608\n"
  "ds_write_addtid_b32 v105 offset:10784\n"
  "s_waitcnt vmcnt(5)\n"
  "v_mfma_f32_16x16x32_bf16 v[12:15], a[116:119], v[160:163], v[12:15]\n"
  "ds_write_addtid_b32 v106 offset:54912\n"
  "ds_write_addtid_b32 v107 offset:61088\n"
  "v_mfma_f32_16x16x32_bf16 v[56:59], a[116:119], v[164:167], v[56:59]\n"
  "ds_write_addtid_b32 v204 offset:768\n"
  "v_mfma_f32_16x16x32_bf16 v[116:119], a[116:119], v[168:171], v[116:119]\n"
  "ds_write_addtid_b32 v205 offset:6944\n"
  "ds_write_addtid_b32 v206 offset:51072\n"
  "v_mfma_f32_16x16x32_bf16 v[8:11], a[80:83], v[24:27], v[8:11]\n"
  "ds_write_addtid_b32 v207 offset:57248\n"
  "v_add_f32_e32 v180, v200, v201\n"
  "v_mov_b32_e32 v181, v214\n"
  "ds_write_b64 v234, v[180:181]\n"
  "v_mfma_f32_16x16x32_bf16 v[52:55], a[80:83], v[28:31], v[52:55]\n"
  "ds_write_addtid_b32 v48 offset:2816\n"
  "ds_write_addtid_b32 v49 offset:8992\n"
  "v_add_f32_e32 v216, v216, v180\n"
  "v_add_f32_e32 v220, v202, v203\n"
  "v_mfma_f32_16x16x32_bf16 v[112:115], a[80:83], v[32:35], v[112:115]\n"
  "ds_write_addtid_b32 v50 offset:53120\n"
  "ds_write_addtid_b32 v51 offset:59296\n"
  "v_add_f32_e32 v216, v216, v220\n"
  "v_mfma_f32_16x16x32_bf16 v[64:67], v[172:175], a[96:99], v[64:67]\n"
  "ds_write_addtid_b32 v108 offset:4864\n"
  "v_cmp_ngt_f32_e32 vcc, s47, v216\n"
  "v_mfma_f32_16x16x32_bf16 v[68:71], v[172:175], a[100:103], v[68:71]\n"
  "ds_write_addtid_b32 v109 offset:11040\n"
  "ds_write_addtid_b32 v110 offset:55168\n"
  "s_waitcnt vmcnt(3)\n"
  "v_mfma_f32_16x16x32_bf16 v[16:19], a[120:123], v[160:163], v[16:19]\n"
  "ds_write_addtid_b32 v111 offset:61344\n"
  "v_mfma_f32_16x16x32_bf16 v[60:63], a[120:123], v[164:167], v[60:63]\n"
  "ds_write_addtid_b32 v8 offset:1024\n"
  "ds_write_addtid_b32 v9 offset:7200\n"
  "v_mfma_f32_16x16x32_bf16 v[120:123], a[120:123], v[168:171], v[120:123]\n"
  "ds_write_addtid_b32 v10 offset:51328\n"
  "ds_write_addtid_b32 v11 offset:57504\n"
  "s_cbranch_vccnz L_redo_%=\n"
  "L_fin_%=:\n"
  "v_accvgpr_read_b32 v131, a3\n"
  "v_accvgpr_read_b32 v128, a0\n"
  "v_accvgpr_read_b32 v129, a1\n"
  "v_accvgpr_read_b32 v130, a2\n"
  "v_accvgpr_read_b32 v132, a4\n"
  "v_mov_b32_e32 v141, 1.0\n"
  "s_mov_b32 s30, 0x400000\n"
  "s_mov_b32 s31, 0x20000\n"
  "s_waitcnt lgkmcnt(4)\n"
  "s_barrier\n"
  "ds_read_b64 v[184:185], v131 offset:0\n"
  "ds_read_b64 v[186:187], v131 offset:1024\n"
  "ds_read_b64 v[188:189], v131 offset:2048\n"
  "v_mfma_f32_16x16x32_bf16 v[12:15], a[84:87], v[24:27], v[12:15]\n"
  "ds_write_addtid_b32 v52 offset:3072\n"
  "ds_read_b128 v[228:231], v128 offset:0\n"
  "v_mfma_f32_16x16x32_bf16 v[56:59], a[84:87], v[28:31], v[56:59]\n"
  "ds_write_addtid_b32 v53 offset:9248\n"
  "ds_write_addtid_b32 v54 offset:53376\n"
  "ds_read_b128 v[4:7], v129 offset:0\n"
  "v_mfma_f32_16x16x32_bf16 v[116:119], a[84:87], v[32:35], v[116:119]\n"
  "ds_write_addtid_b32 v55 offset:59552\n"
  "ds_read_b128 v[96:99], v130 offset:0\n"
  "v_mfma_f32_16x16x32_bf16 v[72:75], v[172:175], a[104:107], v[72:75]\n"
  "ds_write_addtid_b32 v112 offset:5120\n"
  "ds_write_addtid_b32 v113 offset:11296\n"
  "ds_read_b128 v[236:239], v128 offset:256\n"
  "v_mfma_f32_16x16x32_bf16 v[76:79], v[172:175], a[108:111], v[76:79]\n"
  "ds_write_addtid_b32 v114 offset:55424\n"
  "ds_write_addtid_b32 v115 offset:61600\n"
  "ds_read_b128 v[40:43], v129 offset:256\n"
  "s_waitcnt vmcnt(1)\n"
  "v_mfma_f32_16x16x32_bf16 v[20:23], a[124:127], v[160:163], v[20:23]\n"
  "ds_write_addtid_b32 v12 offset:1280\n"
  "ds_read_b128 v[100:103], v130 offset:256\n"
  "v_mfma_f32_16x16x32_bf16 v[196:199], a[124:127], v[164:167], v[196:199]\n"
  "ds_write_addtid_b32 v13 offset:7456\n"
  "ds_write_addtid_b32 v14 offset:51584\n"
  "s_waitcnt lgkmcnt(14)\n"
  "v_max3_f32 v140, v215, v185, v187\n"
  "v_mfma_f32_16x16x32_bf16 v[124:127], a[124:127], v[168:171], v[124:127]\n"
  "ds_write_addtid_b32 v15 offset:57760\n"
  "v_max_f32_e32 v140, v140, v189\n"
  "v_mfma_f32_16x16x32_bf16 v[64:67], v[36:39], a[64:67], v[64:67]\n"
  "ds_write_addtid_b32 v56 offset:3328\n"
  "ds_write_addtid_b32 v57 offset:9504\n"
  "v_sub_f32_e32 v136, v215, v140\n"
  "v_mfma_f32_16x16x32_bf16 v[68:71], v[36:39], a[68:71], v[68:71]\n"
  "ds_write_addtid_b32 v58 offset:53632\n"
  "ds_write_addtid_b32 v59 offset:59808\n"
  "v_sub_f32_e32 v137, v185, v140\n"
  "v_mfma_f32_16x16x32_bf16 v[16:19], a[88:91], v[24:27], v[16:19]\n"
  "ds_write_addtid_b32 v116 offset:5376\n"
  "v_sub_f32_e32 v138, v187, v140\n"
  "v_mfma_f32_16x16x32_bf16 v[60:63], a[88:91], v[28:31], v[60:63]\n"
  "ds_write_addtid_b32 v117 offset:11552\n"
  "ds_write_addtid_b32 v118 offset:55680\n"
  "v_sub_f32_e32 v139, v189, v140\n"
  "v_mfma_f32_16x16x32_bf16 v[120:123], a[88:91], v[32:35], v[120:123]\n"
  "ds_write_addtid_b32 v119 offset:61856\n"
  "v_exp_f32_e32 v136, v136\n"
  "v_mfma_f32_16x16x32_bf16 v[72:75], v[36:39], a[72:75], v[72:75]\n"
  "v_exp_f32_e32 v137, v137\n"
  "v_mfma_f32_16x16x32_bf16 v[76:79], v[36:39], a[76:79], v[76:79]\n"
  "ds_write_addtid_b32 v16 offset:1536\n"
  "ds_write_addtid_b32 v17 offset:7712\n"
  "v_exp_f32_e32 v138, v138\n"
  "s_waitcnt vmcnt(0)\n"
  "v_mfma_f32_16x16x32_bf16 v[20:23], a[92:95], v[24:27], v[20:23]\n"
  "ds_write_addtid_b32 v18 offset:51840\n"
  "ds_write_addtid_b32 v19 offset:58016\n"
  "v_exp_f32_e32 v139, v139\n"
  "v_mfma_f32_16x16x32_bf16 v[196:199], a[92:95], v[28:31], v[196:199]\n"
  "ds_write_addtid_b32 v60 offset:3584\n"
  "v_mul_f32_e32 v140, v136, v220\n"
  "v_mfma_f32_16x16x32_bf16 v[124:127], a[92:95], v[32:35], v[124:127]\n"
  "ds_write_addtid_b32 v61 offset:9760\n"
  "ds_write_addtid_b32 v62 offset:53888\n"
  "v_fmac_f32_e32 v140, v137, v184\n"
  "v_mfma_f32_16x16x32_bf16 v[80:83], v[172:175], a[112:115], v[80:83]\n"
  "ds_write_addtid_b32 v63 offset:60064\n"
  "v_fmac_f32_e32 v140, v138, v186\n"
  "v_mfma_f32_16x16x32_bf16 v[84:87], v[172:175], a[116:119], v[84:87]\n"
  "ds_write_addtid_b32 v120 offset:5632\n"
  "ds_write_addtid_b32 v121 offset:11808\n"
  "v_fmac_f32_e32 v140, v139, v188\n"
  "v_mfma_f32_16x16x32_bf16 v[88:91], v[172:175], a[120:123], v[88:91]\n"
  "ds_write_addtid_b32 v122 offset:55936\n"
  "ds_write_addtid_b32 v123 offset:62112\n"
  "v_rcp_f32_e32 v140, v140\n"
  "v_mfma_f32_16x16x32_bf16 v[92:95], v[172:175], a[124:127], v[92:95]\n"
  "ds_write_addtid_b32 v20 offset:1792\n"
  "v_mov_b32_dpp v136, v137 quad_perm:[0,1,2,3] row_mask:0x2 bank_mask:0xf\n"
  "v_mfma_f32_16x16x32_bf16 v[80:83], v[36:39], a[80:83], v[80:83]\n"
  "ds_write_addtid_b32 v21 offset:7968\n"
  "ds_write_addtid_b32 v22 offset:52096\n"
  "v_mov_b32_dpp v136, v138 quad_perm:[0,1,2,3] row_mask:0x4 bank_mask:0xf\n"
  "v_mfma_f32_16x16x32_bf16 v[84:87], v[36:39], a[84:87], v[84:87]\n"
  "ds_write_addtid_b32 v23 offset:58272\n"
  "v_mov_b32_dpp v136, v139 quad_perm:[0,1,2,3] row_mask:0x8 bank_mask:0xf\n"
  "v_mfma_f32_16x16x32_bf16 v[88:91], v[36:39], a[88:91], v[88:91]\n"
  "ds_write_addtid_b32 v196 offset:3840\n"
  "ds_write_addtid_b32 v197 offset:10016\n"
  "v_mul_f32_e32 v136, v136, v140\n"
  "v_mfma_f32_16x16x32_bf16 v[92:95], v[36:39], a[92:95], v[92:95]\n"
  "ds_write_addtid_b32 v198 offset:54144\n"
  "ds_write_addtid_b32 v199 offset:60320\n"
  "ds_write_addtid_b32 v124 offset:5888\n"
  "ds_write_addtid_b32 v125 offset:12064\n"
  "ds_write_addtid_b32 v126 offset:56192\n"
  "ds_write_addtid_b32 v127 offset:62368\n"
  "v_mfma_f32_16x16x1_4b_f32 v[144:159], v136, v141, 0\n"
  "ds_read_b128 v[240:243], v128 offset:512\n"
  "ds_read_b128 v[44:47], v129 offset:512\n"
  "ds_read_b128 v[104:107], v130 offset:512\n"
  "ds_read_b128 v[204:207], v128 offset:768\n"
  "ds_read_b128 v[48:51], v129 offset:768\n"
  "ds_read_b128 v[108:111], v130 offset:768\n"
  "s_nop 4\n"
  "v_pk_mul_f32 v[64:65], v[64:65], v[144:145]\n"
  "v_pk_mul_f32 v[66:67], v[66:67], v[146:147]\n"
  "v_pk_mul_f32 v[68:69], v[68:69], v[144:145]\n"
  "v_pk_mul_f32 v[70:71], v[70:71], v[146:147]\n"
  "s_waitcnt lgkmcnt(14)\n"
  "v_pk_fma_f32 v[64:65], v[228:229], v[148:149], v[64:65]\n"
  "v_pk_fma_f32 v[66:67], v[230:231], v[150:151], v[66:67]\n"
  "v_pk_fma_f32 v[68:69], v[236:237], v[148:149], v[68:69]\n"
  "v_pk_fma_f32 v[70:71], v[238:239], v[150:151], v[70:71]\n"
  "v_pk_fma_f32 v[64:65], v[4:5], v[152:153], v[64:65]\n"
  "v_pk_fma_f32 v[66:67], v[6:7], v[154:155], v[66:67]\n"
  "v_pk_fma_f32 v[68:69], v[40:41], v[152:153], v[68:69]\n"
  "v_pk_fma_f32 v[70:71], v[42:43], v[154:155], v[70:71]\n"
  "v_pk_fma_f32 v[64:65], v[96:97], v[156:157], v[64:65]\n"
  "v_pk_fma_f32 v[66:67], v[98:99], v[158:159], v[66:67]\n"
  "v_pk_fma_f32 v[68:69], v[100:101], v[156:157], v[68:69]\n"
  "v_pk_fma_f32 v[70:71], v[102:103], v[158:159], v[70:71]\n"
  "v_pk_mul_f32 v[72:73], v[72:73], v[144:145]\n"
  "v_pk_mul_f32 v[74:75], v[74:75], v[146:147]\n"
  "v_pk_mul_f32 v[76:77], v[76:77], v[144:145]\n"
  "v_pk_mul_f32 v[78:79], v[78:79], v[146:147]\n"
  "s_waitcnt lgkmcnt(2)\n"
  "v_pk_fma_f32 v[72:73], v[240:241], v[148:149], v[72:73]\n"
  "v_pk_fma_f32 v[74:75], v[242:243], v[150:151], v[74:75]\n"
  "v_pk_fma_f32 v[76:77], v[204:205], v[148:149], v[76:77]\n"
  "v_pk_fma_f32 v[78:79], v[206:207], v[150:151], v[78:79]\n"
  "s_waitcnt lgkmcnt(1)\n"
  "v_pk_fma_f32 v[72:73], v[44:45], v[152:153], v[72:73]\n"
  "v_pk_fma_f32 v[74:75], v[46:47], v[154:155], v[74:75]\n"
  "v_pk_fma_f32 v[76:77], v[48:49], v[152:153], v[76:77]\n"
  "v_pk_fma_f32 v[78:79], v[50:51], v[154:155], v[78:79]\n"
  "s_waitcnt lgkmcnt(0)\n"
  "v_pk_fma_f32 v[72:73], v[104:105], v[156:157], v[72:73]\n"
  "v_pk_fma_f32 v[74:75], v[106:107], v[158:159], v[74:75]\n"
  "v_pk_fma_f32 v[76:77], v[108:109], v[156:157], v[76:77]\n"
  "v_pk_fma_f32 v[78:79], v[110:111], v[158:159], v[78:79]\n"
  "v_cvt_pk_bf16_f32 v136, v64, v68\n"
  "v_cvt_pk_bf16_f32 v137, v72, v76\n"
  "buffer_store_dwordx2 v[136:137], v132, s[28:31], 0 offen offset:0 sc1\n"
  "v_cvt_pk_bf16_f32 v138, v65, v69\n"
  "v_cvt_pk_bf16_f32 v139, v73, v77\n"
  "buffer_store_dwordx2 v[138:139], v132, s[28:31], 0 offen offset:256 sc1\n"
  "v_cvt_pk_bf16_f32 v140, v66, v70\n"
  "v_cvt_pk_bf16_f32 v141, v74, v78\n"
  "buffer_store_dwordx2 v[140:141], v132, s[28:31], 0 offen offset:512 sc1\n"
  "v_cvt_pk_bf16_f32 v142, v67, v71\n"
  "v_cvt_pk_bf16_f32 v143, v75, v79\n"
  "buffer_store_dwordx2 v[142:143], v132, s[28:31], 0 offen offset:768 sc1\n"
  "s_waitcnt lgkmcnt(0)\n"
  "s_barrier\n"
  "ds_read_b128 v[8:11], v128 offset:1024\n"
  "ds_read_b128 v[52:55], v129 offset:1024\n"
  "ds_read_b128 v[112:115], v130 offset:1024\n"
  "ds_read_b128 v[12:15], v128 offset:1280\n"
  "ds_read_b128 v[56:59], v129 offset:1280\n"
  "ds_read_b128 v[116:119], v130 offset:1280\n"
  "v_pk_mul_f32 v[80:81], v[80:81], v[144:145]\n"
  "ds_read_b128 v[16:19], v128 offset:1536\n"
  "v_pk_mul_f32 v[82:83], v[82:83], v[146:147]\n"
  "ds_read_b128 v[60:63], v129 offset:1536\n"
  "v_pk_mul_f32 v[84:85], v[84:85], v[144:145]\n"
  "ds_read_b128 v[120:123], v130 offset:1536\n"
  "v_pk_mul_f32 v[86:87], v[86:87], v[146:147]\n"
  "ds_read_b128 v[20:23], v128 offset:1792\n"
  "s_waitcnt lgkmcnt(6)\n"
  "v_pk_fma_f32 v[80:81], v[8:9], v[148:149], v[80:81]\n"
  "ds_read_b128 v[196:199], v129 offset:1792\n"
  "v_pk_fma_f32 v[82:83], v[10:11], v[150:151], v[82:83]\n"
  "ds_read_b128 v[124:127], v130 offset:1792\n"
  "v_pk_fma_f32 v[84:85], v[12:13], v[148:149], v[84:85]\n"
  "v_pk_fma_f32 v[86:87], v[14:15], v[150:151], v[86:87]\n"
  "s_waitcnt lgkmcnt(7)\n"
  "v_pk_fma_f32 v[80:81], v[52:53], v[152:153], v[80:81]\n"
  "v_pk_fma_f32 v[82:83], v[54:55], v[154:155], v[82:83]\n"
  "v_pk_fma_f32 v[84:85], v[56:57], v[152:153], v[84:85]\n"
  "v_pk_fma_f32 v[86:87], v[58:59], v[154:155], v[86:87]\n"
  "s_waitcnt lgkmcnt(6)\n"
  "v_pk_fma_f32 v[80:81], v[112:113], v[156:157], v[80:81]\n"
  "v_pk_fma_f32 v[82:83], v[114:115], v[158:159], v[82:83]\n"
  "v_pk_fma_f32 v[84:85], v[116:117], v[156:157], v[84:85]\n"
  "v_pk_fma_f32 v[86:87], v[118:119], v[158:159], v[86:87]\n"
  "v_pk_mul_f32 v[88:89], v[88:89], v[144:145]\n"
  "v_pk_mul_f32 v[90:91], v[90:91], v[146:147]\n"
  "v_pk_mul_f32 v[92:93], v[92:93], v[144:145]\n"
  "v_pk_mul_f32 v[94:95], v[94:95], v[146:147]\n"
  "s_waitcnt lgkmcnt(2)\n"
  "v_pk_fma_f32 v[88:89], v[16:17], v[148:149], v[88:89]\n"
  "v_pk_fma_f32 v[90:91], v[18:19], v[150:151], v[90:91]\n"
  "v_pk_fma_f32 v[92:93], v[20:21], v[148:149], v[92:93]\n"
  "v_pk_fma_f32 v[94:95], v[22:23], v[150:151], v[94:95]\n"
  "s_waitcnt lgkmcnt(1)\n"
  "v_pk_fma_f32 v[88:89], v[60:61], v[152:153], v[88:89]\n"
  "v_pk_fma_f32 v[90:91], v[62:63], v[154:155], v[90:91]\n"
  "v_pk_fma_f32 v[92:93], v[196:197], v[152:153], v[92:93]\n"
  "v_pk_fma_f32 v[94:95], v[198:199], v[154:155], v[94:95]\n"
  "s_waitcnt lgkmcnt(0)\n"
  "v_pk_fma_f32 v[88:89], v[120:121], v[156:157], v[88:89]\n"
  "v_pk_fma_f32 v[90:91], v[122:123], v[158:159], v[90:91]\n"
  "v_pk_fma_f32 v[92:93], v[124:125], v[156:157], v[92:93]\n"
  "v_pk_fma_f32 v[94:95], v[126:127], v[158:159], v[94:95]\n"
  "v_cvt_pk_bf16_f32 v160, v80, v84\n"
  "v_cvt_pk_bf16_f32 v161, v88, v92\n"
  "buffer_store_dwordx2 v[160:161], v132, s[28:31], 0 offen offset:128 sc1\n"
  "v_cvt_pk_bf16_f32 v162, v81, v85\n"
  "v_cvt_pk_bf16_f32 v163, v89, v93\n"
  "buffer_store_dwordx2 v[162:163], v132, s[28:31], 0 offen offset:384 sc1\n"
  "v_cvt_pk_bf16_f32 v164, v82, v86\n"
  "v_cvt_pk_bf16_f32 v165, v90, v94\n"
  "buffer_store_dwordx2 v[164:165], v132, s[28:31], 0 offen offset:640 sc1\n"
  "v_cvt_pk_bf16_f32 v166, v83, v87\n"
  "v_cvt_pk_bf16_f32 v167, v91, v95\n"
  "buffer_store_dwordx2 v[166:167], v132, s[28:31], 0 offen offset:896 sc1\n"
  : "+{v[64:79]}"(f2), "+{v[80:95]}"(f3), "+{v[0:3]}"(f0), "+{v[8:23]}"(f1), "+{s[4:19]}"(sA), "+{s[20:23]}"(sB),
    "={v220}"(lo), "={v215}"(mo), "={s[50:51]}"(tag),
    "+{s[80:95]}"(ka0), "+{s[96:99]}"(ka1), "+{s[72:75]}"(sfl), "+v"(tid), "+{s40}"(kvo), "+{s52}"(flo), "+{v[228:231]}"(qra), "+{v[236:239]}"(pa), "+{v[240:243]}"(pb)
  : "{s[28:29]}"(outp), "{s[36:39]}"(sW), "{s[60:63]}"(ka2), "{v245}"(vfs2),
    "{s49}"(w), "{s56}"((c & ROTM) << 14), "{s[64:67]}"(sraw), "{s[68:71]}"(sws), "{s76}"(h << 19),
    "{a[0:3]}"(cad), "{a4}"(vo), "{v224}"(vk), "{v226}"(vv), "{v[232:235]}"(exa), "{v244}"(vfs),
    "{v246}"(((lane >> 1) & 1) == (lane >> 5) ? 0x3f803f80u : 0u), "{v247}"(lane & 1 ? 0xeeeeu : 0x4444u)
  : "memory", "vcc", "scc", CLOB_S, CLOB_V, CLOB_A);
  lane = tid & 63;
  __attribute__((address_space(1))) char* const wsg = (__attribute__((address_space(1))) char*)(ka2[2] | ((unsigned long long)ka2[3] << 32));
  if ((b & ~8u) == 0 && tid == 0) __hip_atomic_store((gu64*)(wsg + EPOCH_OFF + (b >> 3) * 128), tag, __ATOMIC_RELAXED, __HIP_MEMORY_SCOPE_AGENT);
}

extern "C" void kernel_launch(void* const* d_in, const int* in_sizes, int n_in, void* d_out, int out_size, void* d_ws, size_t ws_size, hipStream_t stream) {
  (void)in_sizes; (void)n_in; (void)out_size; (void)ws_size;
  (void)hipFuncSetAttribute((const void*)fused_kernel, hipFuncAttributeMaxDynamicSharedMemorySize, LDS_B);
  fused_kernel<<<256, 256, LDS_B, stream>>>((const unsigned short*)d_in[0], (const int*)d_in[1], (const unsigned short*)d_in[2], (const unsigned short*)d_in[3],
      (const int*)d_in[4], (const unsigned short*)d_in[5], (const unsigned short*)d_in[6], (const unsigned short*)d_in[7], (const unsigned short*)d_in[8],
      (const unsigned short*)d_in[9], (const unsigned short*)d_in[10], (char*)d_ws, (unsigned short*)d_out);
}
```
